```python
import torch

def _softmax(x, axis=-1):
    return torch.softmax(x, dim=axis)
def _gelu(x):
    return torch.nn.functional.gelu(x, approximate="tanh")
def _silu(x):
    return torch.nn.functional.silu(x)
def _sigmoid(x):
    return torch.sigmoid(x)
def _layernorm(x, axis=-1, eps=1e-5):
    m = x.mean(dim=axis, keepdim=True)
    v = x.var(dim=axis, keepdim=True, unbiased=False)
    return (x - m) / torch.sqrt(v + eps)
def _scatter_add(tgt, idx, src, axis=0):
    return tgt.clone().index_add_(axis, idx, src)
def _segment_sum(src, seg, n):
    out = torch.zeros((n,) + src.shape[1:], dtype=src.dtype)
    return out.index_add_(0, seg, src)
def _pad(x, pw):
    flat = tuple(v for pair in reversed(pw) for v in pair)
    return torch.nn.functional.pad(x, flat)

kernel_name = 'sequential_preLN__ssm_conv_s4d__dense_activation_squared_relu__prompt'

def setup_inputs():

    _w = (lambda a: (lambda t: t.to(torch.bfloat16) if t.is_floating_point() else t)(torch.as_tensor(a)))
    def _A_setup():
        Bsz, L, D, N = 1, 2048, 576, 128
        return {
            "u": _w((lambda *s: torch.randn(*s, dtype=torch.bfloat16) * 0.05)(Bsz, L, D)),
            "log_A_real": _w((lambda *s: torch.randn(*s, dtype=torch.bfloat16) * 3.2)(D, N) * 0.5),
            "A_imag": _w((lambda *s: torch.randn(*s, dtype=torch.bfloat16) * 0.05)(D, N) * 3.14),
            "C_real": _w((lambda *s: torch.randn(*s, dtype=torch.bfloat16) * 0.1)(D, N) / N ** 0.5),
            "C_imag": _w((lambda *s: torch.randn(*s, dtype=torch.bfloat16) * 0.1)(D, N) / N ** 0.5),
            "log_dt": _w((lambda *s: torch.randn(*s, dtype=torch.bfloat16) * 0.05)(D) * 0.5 - 4.0),
            "D_skip": _w((lambda *s: torch.randn(*s, dtype=torch.bfloat16) * 0.05)(D)),
        }
    _A_in = _A_setup()

    def _B_setup():
        B, S, D, F = 1, 2048, 576, 4608
        return {
            "x": _w((lambda *s: torch.randn(*s, dtype=torch.bfloat16) * 0.05)(B, S, D)),
            "w1": _w((lambda *s: torch.randn(*s, dtype=torch.bfloat16) * 0.05)(D, F)),
            "b1": _w((lambda *s: torch.randn(*s, dtype=torch.bfloat16) * 0.2)(F)),
            "w2": _w((lambda *s: torch.randn(*s, dtype=torch.bfloat16) * 0.05)(F, D)),
            "b2": _w((lambda *s: torch.randn(*s, dtype=torch.bfloat16) * 1.6)(D)),
        }
    _B_in = _B_setup()

    return {
        "A_u": _A_in["u"],
        "A_log_A_real": _A_in["log_A_real"],
        "A_A_imag": _A_in["A_imag"],
        "A_C_real": _A_in["C_real"],
        "A_C_imag": _A_in["C_imag"],
        "A_log_dt": _A_in["log_dt"],
        "A_D_skip": _A_in["D_skip"],
        "B_x": _B_in["x"],
        "B_w1": _B_in["w1"],
        "B_b1": _B_in["b1"],
        "B_w2": _B_in["w2"],
        "B_b2": _B_in["b2"],
    }

def reference(A_u, A_log_A_real, A_A_imag, A_C_real, A_C_imag, A_log_dt, A_D_skip, B_x, B_w1, B_b1, B_w2, B_b2):
    def _A_ref(u, log_A_real, A_imag, C_real, C_imag, log_dt, D_skip):
        L = u.shape[1]
        dt = torch.exp(log_dt)
        A = -torch.exp(log_A_real) + 1j * A_imag
        C = (C_real + 1j * C_imag) * dt[:, None]
        pos = (lambda x, dt: x.to(dt))(torch.arange(L), log_dt.dtype)
        P = torch.exp((A * dt[:, None])[:, :, None] * pos[None, None, :])
        K = 2.0 * torch.einsum("dn,dnl->dl", C, P).real
        L2 = 2 * L
        u_t = (lambda x, *d: x.permute(*d))(u, 0, 2, 1)
        Kf = (lambda x, *a, **k: torch.fft.rfft(x.float(), *a, **k))(K, L2)
        Uf = (lambda x, *a, **k: torch.fft.rfft(x.float(), *a, **k))(u_t, L2)
        y = (lambda x, *a, **k: torch.fft.irfft(x, *a, **k).to(torch.bfloat16))(Uf * Kf[None, :, :], L2)[:, :, :L]
        y = (lambda x, *d: x.permute(*d))(y, 0, 2, 1)
        return y + u * D_skip[None, None, :]

    def _B_ref(x, w1, b1, w2, b2):
        h = torch.einsum("bsd,df->bsf", x, w1) + b1
        zero = torch.zeros_like(h)
        r = torch.where(h > zero, h, zero)
        h = r * r
        out = torch.einsum("bsf,fd->bsd", h, w2) + b2
        return out

    x = A_u
    h = x + _A_ref(_layernorm(x, axis=-1), A_log_A_real, A_A_imag, A_C_real, A_C_imag, A_log_dt, A_D_skip)
    h2 = h + _B_ref(_layernorm(h, axis=-1), B_w1, B_b1, B_w2, B_b2)
    return h2
```

```cpp
#include <hip/hip_runtime.h>
#include <hip/hip_bf16.h>
#include <stdint.h>
typedef unsigned short u16;
constexpr int L=2048, D=576, N=128, F=4608;
__device__ __forceinline__ float bf2f(u16 v){ return __uint_as_float(((unsigned)v)<<16); }
__device__ __forceinline__ u16 f2bf(float f){ unsigned u=__float_as_uint(f); u += 0x7FFF + ((u>>16)&1); return (u16)(u>>16); }
__device__ __forceinline__ float rbf(float f){ return bf2f(f2bf(f)); }

__global__ void k_ln(const u16* __restrict__ x, u16* __restrict__ o){
  int r=blockIdx.x, t=threadIdx.x;
  float v[9]; float s=0;
  for(int i=0;i<9;i++){ v[i]=bf2f(x[r*D+t+64*i]); s+=v[i]; }
  for(int o2=32;o2;o2>>=1) s+=__shfl_xor(s,o2);
  float m=s/D; float q=0;
  for(int i=0;i<9;i++){ float d=v[i]-m; q+=d*d; }
  for(int o2=32;o2;o2>>=1) q+=__shfl_xor(q,o2);
  float var=rbf(q/D); float mb=rbf(m);
  float sd=rbf(sqrtf(rbf(var+1e-5f)));
  for(int i=0;i<9;i++){ o[r*D+t+64*i]=f2bf(rbf(v[i]-mb)/sd); }
}
__global__ void k_kgen(const u16* logA,const u16* Aim,const u16* Cr,const u16* Ci,const u16* logdt,float* K){
  int d=blockIdx.x;
  __shared__ float zr[N],zi[N],cr[N],ci[N];
  float dt=rbf(expf(bf2f(logdt[d])));
  for(int n=threadIdx.x;n<N;n+=blockDim.x){
    float a=-rbf(expf(bf2f(logA[d*N+n])));
    zr[n]=a*dt; zi[n]=bf2f(Aim[d*N+n])*dt; cr[n]=bf2f(Cr[d*N+n])*dt; ci[n]=bf2f(Ci[d*N+n])*dt;
  }
  __syncthreads();
  for(int l=threadIdx.x;l<L;l+=blockDim.x){
    float p=rbf((float)l); float s=0;
    for(int n=0;n<N;n++){ float e=expf(zr[n]*p); float sn,cs; sincosf(zi[n]*p,&sn,&cs); s+= cr[n]*(e*cs)-ci[n]*(e*sn); }
    K[d*L+l]=2.f*s;
  }
}
__global__ void k_conv(const float* K,const u16* ln,const u16* x,const u16* Dsk,u16* h){
  int d=blockIdx.x;
  __shared__ float k[L],u[L];
  for(int l=threadIdx.x;l<L;l+=blockDim.x){ k[l]=K[d*L+l]; u[l]=bf2f(ln[l*D+d]); }
  __syncthreads();
  float ds=bf2f(Dsk[d]);
  for(int l=threadIdx.x;l<L;l+=blockDim.x){
    float s=0; for(int j=0;j<=l;j++) s+=k[j]*u[l-j];
    float y=rbf(s); float t=rbf(y+rbf(u[l]*ds));
    h[l*D+d]=f2bf(bf2f(x[l*D+d])+t);
  }
}
__global__ void k_g1(const u16* a,const u16* w,const u16* b,u16* H){
  int m=blockIdx.y; int f=blockIdx.x*256+threadIdx.x;
  __shared__ float s[D];
  for(int i=threadIdx.x;i<D;i+=256) s[i]=bf2f(a[m*D+i]);
  __syncthreads();
  float acc=0; for(int k=0;k<D;k++) acc+=s[k]*bf2f(w[k*F+f]);
  float t=rbf(rbf(acc)+bf2f(b[f])); t=t>0?t:0; H[(size_t)m*F+f]=f2bf(t*t);
}
__global__ void k_g2(const u16* H,const u16* w,const u16* b,const u16* h,u16* out){
  int m=blockIdx.x;
  __shared__ float s[F];
  for(int i=threadIdx.x;i<F;i+=blockDim.x) s[i]=bf2f(H[(size_t)m*F+i]);
  __syncthreads();
  for(int d=threadIdx.x;d<D;d+=blockDim.x){
    float acc=0; for(int k=0;k<F;k++) acc+=s[k]*bf2f(w[k*D+d]);
    out[m*D+d]=f2bf(bf2f(h[m*D+d])+rbf(rbf(acc)+bf2f(b[d])));
  }
}
extern "C" void kernel_launch(void* const* d_in, const int* in_sizes, int n_in,
                              void* d_out, int out_size, void* d_ws, size_t ws_size,
                              hipStream_t stream) {
  const u16* u=(const u16*)d_in[0];
  char* ws=(char*)d_ws;
  u16* ln1=(u16*)ws; u16* h=(u16*)(ws+(4<<20)); u16* ln2=(u16*)(ws+(8<<20)); float* K=(float*)(ws+(12<<20)); u16* H=(u16*)(ws+(32<<20));
  k_ln<<<L,64,0,stream>>>(u,ln1);
  k_kgen<<<D,256,0,stream>>>((const u16*)d_in[1],(const u16*)d_in[2],(const u16*)d_in[3],(const u16*)d_in[4],(const u16*)d_in[5],K);
  k_conv<<<D,256,0,stream>>>(K,ln1,u,(const u16*)d_in[6],h);
  k_ln<<<L,64,0,stream>>>(h,ln2);
  k_g1<<<dim3(F/256,L),256,0,stream>>>(ln2,(const u16*)d_in[8],(const u16*)d_in[9],H);
  k_g2<<<L,576,0,stream>>>(H,(const u16*)d_in[10],(const u16*)d_in[11],h,(u16*)d_out);
}
```

```cpp
#define K2_R2 216
#define CP3_BYTES 23040
#define CPF1 16
#define CPF13 1
#define K1_PF 0
#define CPF2 560
#define K3_G 4
#define RT1_DELAY 24
#define RT2_DELAY 24
#include <hip/hip_runtime.h>
#include <stdint.h>
typedef unsigned short u16;
typedef unsigned int u32;
typedef u32 u32x4 __attribute__((ext_vector_type(4)));
typedef u32 u32x2 __attribute__((ext_vector_type(2)));
typedef float f32x4 __attribute__((ext_vector_type(4)));
constexpr int L_ = 2048, D_ = 576, N_ = 128, F_ = 4608;
__device__ __forceinline__ float bf2f(u16 v) { return __uint_as_float(((u32)v) << 16); }
__device__ __forceinline__ u16 f2bf(float f) { u32 u = __float_as_uint(f); u += 0x7FFF + ((u >> 16) & 1); return (u16)(u >> 16); }
__device__ __forceinline__ float rbf(float f) { return (float)(__bf16)f; }
__device__ __forceinline__ float blo(u32 v) { return __uint_as_float(v << 16); }
__device__ __forceinline__ float bhi(u32 v) { return __uint_as_float(v & 0xffff0000u); }

constexpr size_t WS_WT = 0;
constexpr size_t WT_STREAM = 324 * 1024;
constexpr size_t WS_CB = WS_WT + 32 * WT_STREAM + 65536;
constexpr size_t WS_HT = WS_CB + 8 * 4608 + 4096;
constexpr size_t WS_PART = WS_HT + (size_t)D_ * L_ * 2;
constexpr size_t WS_LNT = WS_PART + (size_t)8 * L_ * D_ * 2;
constexpr size_t WS_XT = WS_LNT + (size_t)D_ * L_ * 2;
constexpr size_t WS_K = WS_XT + (size_t)D_ * L_ * 2;
constexpr size_t WS_KS = WS_K + (size_t)D_ * L_ * 4 + 65536;
constexpr size_t WS_CS = WS_KS + 4096;
constexpr size_t WS_ST = WS_CS + 3 * 4608 * 4 + 4096;
constexpr size_t WS_FLG = WS_ST + 2048 * 8 + 4096;
constexpr size_t WS_HS = WS_FLG + 4 * 4096;
constexpr size_t WS_DBG = WS_HS + 3 * 5120;
constexpr size_t WS_END = WS_DBG;
#define K3_ASM \
  "v_lshrrev_b32 v82, 3, %[lane]\n" \
  "v_and_b32 v83, 7, %[lane]\n" \
  "v_lshrrev_b32 v84, 1, v83\n" \
  "v_bfe_u32 v85, v82, 1, 1\n" \
  "v_xor_b32 v84, v84, v85\n" \
  "v_lshlrev_b32 v84, 5, v84\n" \
  "v_and_b32 v83, 1, v83\n" \
  "v_lshl_or_b32 v84, v83, 4, v84\n" \
  "v_lshl_or_b32 v80, v82, 12, v84\n" \
  "v_xor_b32 v81, 64, v80\n" \
  "v_lshlrev_b32 v8, 4, %[lane]\n" \
  "s_lshl_b32 s43, %[w], 6\n" \
  "v_add_u32 v63, s43, %[lane]\n" \
  "v_min_u32 v62, 143, v63\n" \
  "v_lshlrev_b32 v64, 4, v62\n" \
  "v_lshlrev_b32 v62, 3, v62\n" \
  "s_mov_b32 s43, 18432\n" \
  "s_mov_b32 s44, 36864\n" \
  "buffer_load_dwordx4 v[48:51], v64, %[rsC], 0 offen\n" \
  "buffer_load_dwordx4 v[52:55], v64, %[rsC], s43 offen\n" \
  "buffer_load_dwordx4 v[56:59], v64, %[rsC], s44 offen\n" \
  "buffer_load_dwordx2 v[60:61], v62, %[rsB], 0 offen\n" \
  "s_mul_i32 s43, %[w], 589824\n" \
  "s_mul_i32 s44, %[w], 18432\n" \
  "s_add_u32 m0, s44, %[ldsb]\n" \
  "s_nop 0\n" \
  "buffer_load_dwordx4 v80, %[rsH], s43 offen lds\n" \
  "s_add_u32 m0, m0, 1024\n" \
  "s_add_u32 s43, s43, 32768\n" \
  "buffer_load_dwordx4 v81, %[rsH], s43 offen lds\n" \
  "s_add_u32 m0, m0, 1024\n" \
  "s_add_u32 s43, s43, 32768\n" \
  "buffer_load_dwordx4 v80, %[rsH], s43 offen lds\n" \
  "s_add_u32 m0, m0, 1024\n" \
  "s_add_u32 s43, s43, 32768\n" \
  "buffer_load_dwordx4 v81, %[rsH], s43 offen lds\n" \
  "s_add_u32 m0, m0, 1024\n" \
  "s_add_u32 s43, s43, 32768\n" \
  "buffer_load_dwordx4 v80, %[rsH], s43 offen lds\n" \
  "s_add_u32 m0, m0, 1024\n" \
  "s_add_u32 s43, s43, 32768\n" \
  "buffer_load_dwordx4 v81, %[rsH], s43 offen lds\n" \
  "s_add_u32 m0, m0, 1024\n" \
  "s_add_u32 s43, s43, 32768\n" \
  "buffer_load_dwordx4 v80, %[rsH], s43 offen lds\n" \
  "s_add_u32 m0, m0, 1024\n" \
  "s_add_u32 s43, s43, 32768\n" \
  "buffer_load_dwordx4 v81, %[rsH], s43 offen lds\n" \
  "s_add_u32 m0, m0, 1024\n" \
  "s_add_u32 s43, s43, 32768\n" \
  "buffer_load_dwordx4 v80, %[rsH], s43 offen lds\n" \
  "s_add_u32 m0, m0, 1024\n" \
  "s_add_u32 s43, s43, 32768\n" \
  "buffer_load_dwordx4 v81, %[rsH], s43 offen lds\n" \
  "s_add_u32 m0, m0, 1024\n" \
  "s_add_u32 s43, s43, 32768\n" \
  "buffer_load_dwordx4 v80, %[rsH], s43 offen lds\n" \
  "s_add_u32 m0, m0, 1024\n" \
  "s_add_u32 s43, s43, 32768\n" \
  "buffer_load_dwordx4 v81, %[rsH], s43 offen lds\n" \
  "s_add_u32 m0, m0, 1024\n" \
  "s_add_u32 s43, s43, 32768\n" \
  "buffer_load_dwordx4 v80, %[rsH], s43 offen lds\n" \
  "s_add_u32 m0, m0, 1024\n" \
  "s_add_u32 s43, s43, 32768\n" \
  "buffer_load_dwordx4 v81, %[rsH], s43 offen lds\n" \
  "s_add_u32 m0, m0, 1024\n" \
  "s_add_u32 s43, s43, 32768\n" \
  "buffer_load_dwordx4 v80, %[rsH], s43 offen lds\n" \
  "s_add_u32 m0, m0, 1024\n" \
  "s_add_u32 s43, s43, 32768\n" \
  "buffer_load_dwordx4 v81, %[rsH], s43 offen lds\n" \
  "s_add_u32 m0, m0, 1024\n" \
  "s_add_u32 s43, s43, 32768\n" \
  "buffer_load_dwordx4 v80, %[rsH], s43 offen lds\n" \
  "s_add_u32 m0, m0, 1024\n" \
  "s_add_u32 s43, s43, 32768\n" \
  "buffer_load_dwordx4 v81, %[rsH], s43 offen lds\n" \
  "v_add_u32 v9, 4096, v8\n" \
  "v_add_u32 v10, 8192, v8\n" \
  "v_add_u32 v11, 12288, v8\n" \
  "v_add_u32 v12, 16384, v8\n" \
  "s_mov_b32 s40, 0\n" \
  "buffer_load_dwordx4 v[184:187], v8, %[rsW], s40 offen sc1\n" \
  "buffer_load_dwordx4 v[188:191], v8, %[rsW], s40 offen offset:1024 sc1\n" \
  "buffer_load_dwordx4 v[192:195], v8, %[rsW], s40 offen offset:2048 sc1\n" \
  "buffer_load_dwordx4 v[196:199], v8, %[rsW], s40 offen offset:3072 sc1\n" \
  "buffer_load_dwordx4 v[200:203], v9, %[rsW], s40 offen sc1\n" \
  "buffer_load_dwordx4 v[204:207], v9, %[rsW], s40 offen offset:1024 sc1\n" \
  "v_and_b32 v82, 15, %[lane]\n" \
  "v_lshrrev_b32 v83, 4, %[lane]\n" \
  "v_bfe_u32 v84, %[lane], 2, 2\n" \
  "v_and_b32 v85, 3, %[lane]\n" \
  "v_lshl_add_u32 v86, v83, 3, v84\n" \
  "v_lshlrev_b32 v86, 7, v86\n" \
  "v_lshl_add_u32 v86, v85, 3, v86\n" \
  "v_lshrrev_b32 v87, 1, v84\n" \
  "v_and_b32 v39, 1, v83\n" \
  "v_lshl_or_b32 v87, v39, 1, v87\n" \
  "v_xor_b32 v87, %[w], v87\n" \
  "v_lshl_add_u32 v86, v87, 5, v86\n" \
  "v_add_u32 v38, %[ldsb], v86\n" \
  "v_xor_b32 v39, 32, v38\n" \
  "v_xor_b32 v40, 64, v38\n" \
  "v_xor_b32 v41, 96, v38\n" \
  "v_add_u32 v42, 65536, v38\n" \
  "v_add_u32 v43, 65536, v39\n" \
  "v_add_u32 v44, 65536, v40\n" \
  "v_add_u32 v45, 65536, v41\n" \
  "s_mul_i32 s43, %[w], 17408\n" \
  "s_add_u32 s43, s43, %[ldsb]\n" \
  "s_add_u32 s43, s43, 73728\n" \
  "v_add_u32 v17, s43, v8\n" \
  "v_xor_b32 v18, 1024, v17\n" \
  "v_xor_b32 v19, 2048, v17\n" \
  "v_xor_b32 v20, 3072, v17\n" \
  "s_mul_i32 s43, %[w], 576\n" \
  "s_add_u32 s43, s43, %[ldsb]\n" \
  "s_add_u32 s43, s43, 147456\n" \
  "v_lshl_add_u32 v29, v83, 4, s43\n" \
  "s_lshl_b32 s43, %[w], 7\n" \
  "s_add_u32 s43, s43, %[ldsb]\n" \
  "s_add_u32 s43, s43, 152576\n" \
  "v_lshl_add_u32 v24, v82, 3, s43\n" \
  "v_mov_b32 v104, 0x3f803f80\n" \
  "v_mov_b32 v105, 0x3f803f80\n" \
  "v_mov_b32 v106, 0x3f803f80\n" \
  "v_mov_b32 v107, 0x3f803f80\n" \
  "s_mov_b32 s46, 0x22222222\n" \
  "s_mov_b32 s47, 0x22222222\n" \
  "s_mov_b32 s48, 0x44444444\n" \
  "s_mov_b32 s49, 0x44444444\n" \
  "s_mov_b32 s50, 0x88888888\n" \
  "s_mov_b32 s51, 0x88888888\n" \
  "s_mov_b32 s42, 0\n" \
  "s_waitcnt vmcnt(24)\n" \
  "v_add_f32 v48, v48, v52\n" \
  "v_add_f32 v49, v49, v53\n" \
  "v_add_f32 v50, v50, v54\n" \
  "v_add_f32 v51, v51, v55\n" \
  "v_lshlrev_b32 v52, 16, v60\n" \
  "v_and_b32 v53, 0xffff0000, v60\n" \
  "v_lshlrev_b32 v54, 16, v61\n" \
  "v_and_b32 v55, 0xffff0000, v61\n" \
  "v_add_f32 v48, v48, v56\n" \
  "v_add_f32 v49, v49, v57\n" \
  "v_add_f32 v50, v50, v58\n" \
  "v_add_f32 v51, v51, v59\n" \
  "s_add_u32 s43, %[ldsb], 147456\n" \
  "v_add_u32 v64, s43, v64\n" \
  "v_cmp_gt_u32 vcc, 144, v63\n" \
  "s_nop 4\n" \
  "s_and_b64 exec, exec, vcc\n" \
  "s_cbranch_execz L_cbskip_%=\n" \
  "ds_write_b128 v64, v[48:51]\n" \
  "ds_write_b128 v64, v[52:55] offset:2304\n" \
  "L_cbskip_%=:\n" \
  "s_mov_b64 exec, -1\n" \
  "s_waitcnt vmcnt(6) lgkmcnt(0)\n" \
  "s_barrier\n" \
  "ds_read_b64_tr_b16 a[0:1], v38 offset:0\n" \
  "ds_read_b64_tr_b16 a[2:3], v38 offset:512\n" \
  "ds_read_b64_tr_b16 a[16:17], v38 offset:4096\n" \
  "ds_read_b64_tr_b16 a[18:19], v38 offset:4608\n" \
  "ds_read_b64_tr_b16 a[32:33], v38 offset:8192\n" \
  "ds_read_b64_tr_b16 a[34:35], v38 offset:8704\n" \
  "ds_read_b64_tr_b16 a[48:49], v38 offset:12288\n" \
  "ds_read_b64_tr_b16 a[50:51], v38 offset:12800\n" \
  "ds_read_b64_tr_b16 a[64:65], v38 offset:16384\n" \
  "ds_read_b64_tr_b16 a[66:67], v38 offset:16896\n" \
  "ds_read_b64_tr_b16 a[80:81], v38 offset:20480\n" \
  "ds_read_b64_tr_b16 a[82:83], v38 offset:20992\n" \
  "ds_read_b64_tr_b16 a[96:97], v38 offset:24576\n" \
  "ds_read_b64_tr_b16 a[98:99], v38 offset:25088\n" \
  "s_waitcnt lgkmcnt(12)\n" \
  "v_mfma_f32_16x16x32_bf16 v[96:99], v[104:107], a[0:3], 0\n" \
  "ds_read_b64_tr_b16 a[112:113], v38 offset:28672\n" \
  "ds_read_b64_tr_b16 a[114:115], v38 offset:29184\n" \
  "buffer_load_dwordx4 v[208:211], v9, %[rsW], s40 offen offset:2048 sc1\n" \
  "v_mfma_f32_16x16x32_bf16 v[100:103], a[0:3], a[0:3], 0\n" \
  "ds_read_b64_tr_b16 a[128:129], v38 offset:32768\n" \
  "s_waitcnt lgkmcnt(9)\n" \
  "ds_read_b64_tr_b16 a[130:131], v38 offset:33280\n" \
  "buffer_load_dwordx4 v[212:215], v9, %[rsW], s40 offen offset:3072 sc1\n" \
  "v_mfma_f32_16x16x32_bf16 v[96:99], v[104:107], a[16:19], v[96:99]\n" \
  "ds_read_b64_tr_b16 a[144:145], v38 offset:36864\n" \
  "ds_read_b64_tr_b16 a[146:147], v38 offset:37376\n" \
  "buffer_load_dwordx4 v[216:219], v10, %[rsW], s40 offen sc1\n" \
  "v_mfma_f32_16x16x32_bf16 v[100:103], a[16:19], a[16:19], v[100:103]\n" \
  "ds_read_b64_tr_b16 a[160:161], v38 offset:40960\n" \
  "ds_read_b64_tr_b16 a[162:163], v38 offset:41472\n" \
  "buffer_load_dwordx4 v[220:223], v10, %[rsW], s40 offen offset:1024 sc1\n" \
  "v_mfma_f32_16x16x32_bf16 v[96:99], v[104:107], a[32:35], v[96:99]\n" \
  "ds_read_b64_tr_b16 a[176:177], v38 offset:45056\n" \
  "s_waitcnt lgkmcnt(9)\n" \
  "ds_read_b64_tr_b16 a[178:179], v38 offset:45568\n" \
  "buffer_load_dwordx4 v[224:227], v10, %[rsW], s40 offen offset:2048 sc1\n" \
  "v_mfma_f32_16x16x32_bf16 v[100:103], a[32:35], a[32:35], v[100:103]\n" \
  "ds_read_b64_tr_b16 a[192:193], v38 offset:49152\n" \
  "ds_read_b64_tr_b16 a[194:195], v38 offset:49664\n" \
  "buffer_load_dwordx4 v[228:231], v10, %[rsW], s40 offen offset:3072 sc1\n" \
  "v_mfma_f32_16x16x32_bf16 v[96:99], v[104:107], a[48:51], v[96:99]\n" \
  "ds_read_b64_tr_b16 a[208:209], v38 offset:53248\n" \
  "ds_read_b64_tr_b16 a[210:211], v38 offset:53760\n" \
  "buffer_load_dwordx4 v[232:235], v11, %[rsW], s40 offen sc1\n" \
  "v_mfma_f32_16x16x32_bf16 v[100:103], a[48:51], a[48:51], v[100:103]\n" \
  "ds_read_b64_tr_b16 a[224:225], v38 offset:57344\n" \
  "s_waitcnt lgkmcnt(9)\n" \
  "ds_read_b64_tr_b16 a[226:227], v38 offset:57856\n" \
  "buffer_load_dwordx4 v[236:239], v11, %[rsW], s40 offen offset:1024 sc1\n" \
  "v_mfma_f32_16x16x32_bf16 v[96:99], v[104:107], a[64:67], v[96:99]\n" \
  "ds_read_b64_tr_b16 a[240:241], v38 offset:61440\n" \
  "ds_read_b64_tr_b16 a[242:243], v38 offset:61952\n" \
  "buffer_load_dwordx4 v[240:243], v11, %[rsW], s40 offen offset:2048 sc1\n" \
  "v_mfma_f32_16x16x32_bf16 v[100:103], a[64:67], a[64:67], v[100:103]\n" \
  "ds_read_b64_tr_b16 v[48:49], v42 offset:0\n" \
  "ds_read_b64_tr_b16 v[50:51], v42 offset:512\n" \
  "buffer_load_dwordx4 v[244:247], v11, %[rsW], s40 offen offset:3072 sc1\n" \
  "v_mfma_f32_16x16x32_bf16 v[96:99], v[104:107], a[80:83], v[96:99]\n" \
  "ds_read_b64_tr_b16 v[64:65], v42 offset:4096\n" \
  "s_waitcnt lgkmcnt(9)\n" \
  "ds_read_b64_tr_b16 v[66:67], v42 offset:4608\n" \
  "buffer_load_dwordx4 v[248:251], v12, %[rsW], s40 offen sc1\n" \
  "v_mfma_f32_16x16x32_bf16 v[100:103], a[80:83], a[80:83], v[100:103]\n" \
  "ds_read_b64_tr_b16 a[4:5], v39 offset:0\n" \
  "ds_read_b64_tr_b16 a[6:7], v39 offset:512\n" \
  "buffer_load_dwordx4 v[252:255], v12, %[rsW], s40 offen offset:1024 sc1\n" \
  "v_mfma_f32_16x16x32_bf16 v[96:99], v[104:107], a[96:99], v[96:99]\n" \
  "ds_read_b64_tr_b16 a[8:9], v40 offset:0\n" \
  "ds_read_b64_tr_b16 a[10:11], v40 offset:512\n" \
  "v_mfma_f32_16x16x32_bf16 v[100:103], a[96:99], a[96:99], v[100:103]\n" \
  "ds_read_b64_tr_b16 a[12:13], v41 offset:0\n" \
  "s_waitcnt lgkmcnt(9)\n" \
  "ds_read_b64_tr_b16 a[14:15], v41 offset:512\n" \
  "v_mfma_f32_16x16x32_bf16 v[96:99], v[104:107], a[112:115], v[96:99]\n" \
  "ds_read_b64_tr_b16 a[20:21], v39 offset:4096\n" \
  "ds_read_b64_tr_b16 a[22:23], v39 offset:4608\n" \
  "v_mfma_f32_16x16x32_bf16 v[100:103], a[112:115], a[112:115], v[100:103]\n" \
  "ds_read_b64_tr_b16 a[24:25], v40 offset:4096\n" \
  "ds_read_b64_tr_b16 a[26:27], v40 offset:4608\n" \
  "v_mfma_f32_16x16x32_bf16 v[96:99], v[104:107], a[128:131], v[96:99]\n" \
  "ds_read_b64_tr_b16 a[28:29], v41 offset:4096\n" \
  "s_waitcnt lgkmcnt(9)\n" \
  "ds_read_b64_tr_b16 a[30:31], v41 offset:4608\n" \
  "v_mfma_f32_16x16x32_bf16 v[100:103], a[128:131], a[128:131], v[100:103]\n" \
  "ds_read_b64_tr_b16 a[36:37], v39 offset:8192\n" \
  "ds_read_b64_tr_b16 a[38:39], v39 offset:8704\n" \
  "v_mfma_f32_16x16x32_bf16 v[96:99], v[104:107], a[144:147], v[96:99]\n" \
  "ds_read_b64_tr_b16 a[40:41], v40 offset:8192\n" \
  "ds_read_b64_tr_b16 a[42:43], v40 offset:8704\n" \
  "v_mfma_f32_16x16x32_bf16 v[100:103], a[144:147], a[144:147], v[100:103]\n" \
  "ds_read_b64_tr_b16 a[44:45], v41 offset:8192\n" \
  "s_waitcnt lgkmcnt(9)\n" \
  "ds_read_b64_tr_b16 a[46:47], v41 offset:8704\n" \
  "v_mfma_f32_16x16x32_bf16 v[96:99], v[104:107], a[160:163], v[96:99]\n" \
  "ds_read_b64_tr_b16 a[52:53], v39 offset:12288\n" \
  "ds_read_b64_tr_b16 a[54:55], v39 offset:12800\n" \
  "v_mfma_f32_16x16x32_bf16 v[100:103], a[160:163], a[160:163], v[100:103]\n" \
  "ds_read_b64_tr_b16 a[56:57], v40 offset:12288\n" \
  "ds_read_b64_tr_b16 a[58:59], v40 offset:12800\n" \
  "v_mfma_f32_16x16x32_bf16 v[96:99], v[104:107], a[176:179], v[96:99]\n" \
  "ds_read_b64_tr_b16 a[60:61], v41 offset:12288\n" \
  "s_waitcnt lgkmcnt(9)\n" \
  "ds_read_b64_tr_b16 a[62:63], v41 offset:12800\n" \
  "v_mfma_f32_16x16x32_bf16 v[100:103], a[176:179], a[176:179], v[100:103]\n" \
  "ds_read_b64_tr_b16 a[68:69], v39 offset:16384\n" \
  "ds_read_b64_tr_b16 a[70:71], v39 offset:16896\n" \
  "v_mfma_f32_16x16x32_bf16 v[96:99], v[104:107], a[192:195], v[96:99]\n" \
  "ds_read_b64_tr_b16 a[72:73], v40 offset:16384\n" \
  "ds_read_b64_tr_b16 a[74:75], v40 offset:16896\n" \
  "v_mfma_f32_16x16x32_bf16 v[100:103], a[192:195], a[192:195], v[100:103]\n" \
  "ds_read_b64_tr_b16 a[76:77], v41 offset:16384\n" \
  "s_waitcnt lgkmcnt(9)\n" \
  "ds_read_b64_tr_b16 a[78:79], v41 offset:16896\n" \
  "v_mfma_f32_16x16x32_bf16 v[96:99], v[104:107], a[208:211], v[96:99]\n" \
  "ds_read_b64_tr_b16 a[84:85], v39 offset:20480\n" \
  "ds_read_b64_tr_b16 a[86:87], v39 offset:20992\n" \
  "v_mfma_f32_16x16x32_bf16 v[100:103], a[208:211], a[208:211], v[100:103]\n" \
  "ds_read_b64_tr_b16 a[88:89], v40 offset:20480\n" \
  "ds_read_b64_tr_b16 a[90:91], v40 offset:20992\n" \
  "v_mfma_f32_16x16x32_bf16 v[96:99], v[104:107], a[224:227], v[96:99]\n" \
  "ds_read_b64_tr_b16 a[92:93], v41 offset:20480\n" \
  "s_waitcnt lgkmcnt(9)\n" \
  "ds_read_b64_tr_b16 a[94:95], v41 offset:20992\n" \
  "v_mfma_f32_16x16x32_bf16 v[100:103], a[224:227], a[224:227], v[100:103]\n" \
  "ds_read_b64_tr_b16 a[100:101], v39 offset:24576\n" \
  "ds_read_b64_tr_b16 a[102:103], v39 offset:25088\n" \
  "v_mfma_f32_16x16x32_bf16 v[96:99], v[104:107], a[240:243], v[96:99]\n" \
  "ds_read_b64_tr_b16 a[104:105], v40 offset:24576\n" \
  "ds_read_b64_tr_b16 a[106:107], v40 offset:25088\n" \
  "v_mfma_f32_16x16x32_bf16 v[100:103], a[240:243], a[240:243], v[100:103]\n" \
  "ds_read_b64_tr_b16 a[108:109], v41 offset:24576\n" \
  "s_waitcnt lgkmcnt(9)\n" \
  "ds_read_b64_tr_b16 a[110:111], v41 offset:25088\n" \
  "v_mfma_f32_16x16x32_bf16 v[96:99], v[104:107], v[48:51], v[96:99]\n" \
  "ds_read_b64_tr_b16 a[116:117], v39 offset:28672\n" \
  "ds_read_b64_tr_b16 a[118:119], v39 offset:29184\n" \
  "v_mfma_f32_16x16x32_bf16 v[100:103], v[48:51], v[48:51], v[100:103]\n" \
  "ds_read_b64_tr_b16 a[120:121], v40 offset:28672\n" \
  "ds_read_b64_tr_b16 a[122:123], v40 offset:29184\n" \
  "v_mfma_f32_16x16x32_bf16 v[96:99], v[104:107], v[64:67], v[96:99]\n" \
  "ds_read_b64_tr_b16 a[124:125], v41 offset:28672\n" \
  "s_waitcnt lgkmcnt(9)\n" \
  "ds_read_b64_tr_b16 a[126:127], v41 offset:29184\n" \
  "v_mfma_f32_16x16x32_bf16 v[100:103], v[64:67], v[64:67], v[100:103]\n" \
  "ds_read_b64_tr_b16 a[132:133], v39 offset:32768\n" \
  "ds_read_b64_tr_b16 a[134:135], v39 offset:33280\n" \
  "s_waitcnt vmcnt(17)\n" \
  "v_mfma_f32_16x16x32_bf16 v[80:83], v[184:187], a[0:3], 0\n" \
  "v_mfma_f32_16x16x32_bf16 v[84:87], v[184:187], a[4:7], 0\n" \
  "ds_read_b64_tr_b16 a[136:137], v40 offset:32768\n" \
  "ds_read_b64_tr_b16 a[138:139], v40 offset:33280\n" \
  "ds_read_b64_tr_b16 a[140:141], v41 offset:32768\n" \
  "v_mfma_f32_16x16x32_bf16 v[88:91], v[184:187], a[8:11], 0\n" \
  "s_waitcnt vmcnt(16)\n" \
  "v_mfma_f32_16x16x32_bf16 v[92:95], v[184:187], a[12:15], 0\n" \
  "s_waitcnt lgkmcnt(9)\n" \
  "ds_read_b64_tr_b16 a[142:143], v41 offset:33280\n" \
  "ds_read_b64_tr_b16 a[148:149], v39 offset:36864\n" \
  "ds_read_b64_tr_b16 a[150:151], v39 offset:37376\n" \
  "v_mfma_f32_16x16x32_bf16 v[80:83], v[188:191], a[16:19], v[80:83]\n" \
  "s_add_u32 s40, s40, 18432\n" \
  "buffer_load_dwordx4 v[184:187], v8, %[rsW], s40 offen sc1\n" \
  "v_mfma_f32_16x16x32_bf16 v[84:87], v[188:191], a[20:23], v[84:87]\n" \
  "ds_read_b64_tr_b16 a[152:153], v40 offset:36864\n" \
  "ds_read_b64_tr_b16 a[154:155], v40 offset:37376\n" \
  "ds_read_b64_tr_b16 a[156:157], v41 offset:36864\n" \
  "v_mfma_f32_16x16x32_bf16 v[88:91], v[188:191], a[24:27], v[88:91]\n" \
  "s_waitcnt vmcnt(16)\n" \
  "v_mfma_f32_16x16x32_bf16 v[92:95], v[188:191], a[28:31], v[92:95]\n" \
  "s_waitcnt lgkmcnt(9)\n" \
  "ds_read_b64_tr_b16 a[158:159], v41 offset:37376\n" \
  "ds_read_b64_tr_b16 a[164:165], v39 offset:40960\n" \
  "ds_read_b64_tr_b16 a[166:167], v39 offset:41472\n" \
  "v_mfma_f32_16x16x32_bf16 v[80:83], v[192:195], a[32:35], v[80:83]\n" \
  "buffer_load_dwordx4 v[188:191], v8, %[rsW], s40 offen offset:1024 sc1\n" \
  "v_mfma_f32_16x16x32_bf16 v[84:87], v[192:195], a[36:39], v[84:87]\n" \
  "v_cndmask_b32 v108, v100, v101, s[46:47]\n" \
  "ds_read_b64_tr_b16 a[168:169], v40 offset:40960\n" \
  "ds_read_b64_tr_b16 a[170:171], v40 offset:41472\n" \
  "ds_read_b64_tr_b16 a[172:173], v41 offset:40960\n" \
  "v_mfma_f32_16x16x32_bf16 v[88:91], v[192:195], a[40:43], v[88:91]\n" \
  "s_waitcnt vmcnt(16)\n" \
  "v_cndmask_b32 v108, v108, v102, s[48:49]\n" \
  "v_mfma_f32_16x16x32_bf16 v[92:95], v[192:195], a[44:47], v[92:95]\n" \
  "v_cndmask_b32 v108, v108, v103, s[50:51]\n" \
  "s_waitcnt lgkmcnt(9)\n" \
  "ds_read_b64_tr_b16 a[174:175], v41 offset:41472\n" \
  "ds_read_b64_tr_b16 a[180:181], v39 offset:45056\n" \
  "ds_read_b64_tr_b16 a[182:183], v39 offset:45568\n" \
  "v_mfma_f32_16x16x32_bf16 v[80:83], v[196:199], a[48:51], v[80:83]\n" \
  "buffer_load_dwordx4 v[192:195], v8, %[rsW], s40 offen offset:2048 sc1\n" \
  "v_mfma_f32_16x16x32_bf16 v[84:87], v[196:199], a[52:55], v[84:87]\n" \
  "v_mul_f32 v109, 0x3ae38e39, v96\n" \
  "ds_read_b64_tr_b16 a[184:185], v40 offset:45056\n" \
  "ds_read_b64_tr_b16 a[186:187], v40 offset:45568\n" \
  "ds_read_b64_tr_b16 a[188:189], v41 offset:45056\n" \
  "v_mfma_f32_16x16x32_bf16 v[88:91], v[196:199], a[56:59], v[88:91]\n" \
  "s_waitcnt vmcnt(16)\n" \
  "v_mul_f32 v108, 0x3ae38e39, v108\n" \
  "v_mfma_f32_16x16x32_bf16 v[92:95], v[196:199], a[60:63], v[92:95]\n" \
  "v_fma_f32 v108, -v109, v109, v108\n" \
  "s_waitcnt lgkmcnt(9)\n" \
  "ds_read_b64_tr_b16 a[190:191], v41 offset:45568\n" \
  "ds_read_b64_tr_b16 a[196:197], v39 offset:49152\n" \
  "ds_read_b64_tr_b16 a[198:199], v39 offset:49664\n" \
  "v_mfma_f32_16x16x32_bf16 v[80:83], v[200:203], a[64:67], v[80:83]\n" \
  "buffer_load_dwordx4 v[196:199], v8, %[rsW], s40 offen offset:3072 sc1\n" \
  "v_mfma_f32_16x16x32_bf16 v[84:87], v[200:203], a[68:71], v[84:87]\n" \
  "v_max_f32 v108, 0, v108\n" \
  "ds_read_b64_tr_b16 a[200:201], v40 offset:49152\n" \
  "ds_read_b64_tr_b16 a[202:203], v40 offset:49664\n" \
  "ds_read_b64_tr_b16 a[204:205], v41 offset:49152\n" \
  "v_mfma_f32_16x16x32_bf16 v[88:91], v[200:203], a[72:75], v[88:91]\n" \
  "s_waitcnt vmcnt(16)\n" \
  "v_cvt_pk_bf16_f32 v108, v108, v109\n" \
  "v_mfma_f32_16x16x32_bf16 v[92:95], v[200:203], a[76:79], v[92:95]\n" \
  "v_and_b32 v109, 0xffff0000, v108\n" \
  "s_waitcnt lgkmcnt(9)\n" \
  "ds_read_b64_tr_b16 a[206:207], v41 offset:49664\n" \
  "ds_read_b64_tr_b16 a[212:213], v39 offset:53248\n" \
  "ds_read_b64_tr_b16 a[214:215], v39 offset:53760\n" \
  "v_mfma_f32_16x16x32_bf16 v[80:83], v[204:207], a[80:83], v[80:83]\n" \
  "buffer_load_dwordx4 v[200:203], v9, %[rsW], s40 offen sc1\n" \
  "v_mfma_f32_16x16x32_bf16 v[84:87], v[204:207], a[84:87], v[84:87]\n" \
  "v_lshlrev_b32 v108, 16, v108\n" \
  "ds_read_b64_tr_b16 a[216:217], v40 offset:53248\n" \
  "ds_read_b64_tr_b16 a[218:219], v40 offset:53760\n" \
  "ds_read_b64_tr_b16 a[220:221], v41 offset:53248\n" \
  "v_mfma_f32_16x16x32_bf16 v[88:91], v[204:207], a[88:91], v[88:91]\n" \
  "s_waitcnt vmcnt(16)\n" \
  "v_add_f32 v108, 0x3727c5ac, v108\n" \
  "v_mfma_f32_16x16x32_bf16 v[92:95], v[204:207], a[92:95], v[92:95]\n" \
  "v_cvt_pk_bf16_f32 v108, v108, v108\n" \
  "s_waitcnt lgkmcnt(9)\n" \
  "ds_read_b64_tr_b16 a[222:223], v41 offset:53760\n" \
  "ds_read_b64_tr_b16 a[228:229], v39 offset:57344\n" \
  "ds_read_b64_tr_b16 a[230:231], v39 offset:57856\n" \
  "v_mfma_f32_16x16x32_bf16 v[80:83], v[208:211], a[96:99], v[80:83]\n" \
  "buffer_load_dwordx4 v[204:207], v9, %[rsW], s40 offen offset:1024 sc1\n" \
  "v_mfma_f32_16x16x32_bf16 v[84:87], v[208:211], a[100:103], v[84:87]\n" \
  "v_lshlrev_b32 v108, 16, v108\n" \
  "ds_read_b64_tr_b16 a[232:233], v40 offset:57344\n" \
  "ds_read_b64_tr_b16 a[234:235], v40 offset:57856\n" \
  "ds_read_b64_tr_b16 a[236:237], v41 offset:57344\n" \
  "v_mfma_f32_16x16x32_bf16 v[88:91], v[208:211], a[104:107], v[88:91]\n" \
  "s_waitcnt vmcnt(16)\n" \
  "v_sqrt_f32 v108, v108\n" \
  "v_mfma_f32_16x16x32_bf16 v[92:95], v[208:211], a[108:111], v[92:95]\n" \
  "s_nop 0\n" \
  "s_waitcnt lgkmcnt(9)\n" \
  "ds_read_b64_tr_b16 a[238:239], v41 offset:57856\n" \
  "ds_read_b64_tr_b16 a[244:245], v39 offset:61440\n" \
  "ds_read_b64_tr_b16 a[246:247], v39 offset:61952\n" \
  "v_mfma_f32_16x16x32_bf16 v[80:83], v[212:215], a[112:115], v[80:83]\n" \
  "buffer_load_dwordx4 v[208:211], v9, %[rsW], s40 offen offset:2048 sc1\n" \
  "v_mfma_f32_16x16x32_bf16 v[84:87], v[212:215], a[116:119], v[84:87]\n" \
  "v_cvt_pk_bf16_f32 v108, v108, v108\n" \
  "ds_read_b64_tr_b16 a[248:249], v40 offset:61440\n" \
  "ds_read_b64_tr_b16 a[250:251], v40 offset:61952\n" \
  "ds_read_b64_tr_b16 a[252:253], v41 offset:61440\n" \
  "v_mfma_f32_16x16x32_bf16 v[88:91], v[212:215], a[120:123], v[88:91]\n" \
  "s_waitcnt vmcnt(16)\n" \
  "v_lshlrev_b32 v108, 16, v108\n" \
  "v_mfma_f32_16x16x32_bf16 v[92:95], v[212:215], a[124:127], v[92:95]\n" \
  "v_rcp_f32 v110, v108\n" \
  "s_waitcnt lgkmcnt(9)\n" \
  "ds_read_b64_tr_b16 a[254:255], v41 offset:61952\n" \
  "ds_read_b64_tr_b16 v[52:53], v43 offset:0\n" \
  "ds_read_b64_tr_b16 v[54:55], v43 offset:512\n" \
  "v_mfma_f32_16x16x32_bf16 v[80:83], v[216:219], a[128:131], v[80:83]\n" \
  "buffer_load_dwordx4 v[212:215], v9, %[rsW], s40 offen offset:3072 sc1\n" \
  "v_mfma_f32_16x16x32_bf16 v[84:87], v[216:219], a[132:135], v[84:87]\n" \
  "s_nop 0\n" \
  "ds_read_b64_tr_b16 v[56:57], v44 offset:0\n" \
  "ds_read_b64_tr_b16 v[58:59], v44 offset:512\n" \
  "ds_read_b64_tr_b16 v[60:61], v45 offset:0\n" \
  "v_mfma_f32_16x16x32_bf16 v[88:91], v[216:219], a[136:139], v[88:91]\n" \
  "s_waitcnt vmcnt(16)\n" \
  "v_mul_f32 v111, v109, v110\n" \
  "v_mfma_f32_16x16x32_bf16 v[92:95], v[216:219], a[140:143], v[92:95]\n" \
  "s_mov_b32 exec_lo, 0x00f0000f\n" \
  "s_mov_b32 exec_hi, 0xf0000f00\n" \
  "s_waitcnt lgkmcnt(14)\n" \
  "ds_write_b64 v24, v[110:111]\n" \
  "s_mov_b64 exec, -1\n" \
  "s_nop 3\n" \
  "s_waitcnt lgkmcnt(9)\n" \
  "ds_read_b64_tr_b16 v[62:63], v45 offset:512\n" \
  "ds_read_b64_tr_b16 v[68:69], v43 offset:4096\n" \
  "ds_read_b64_tr_b16 v[70:71], v43 offset:4608\n" \
  "v_mfma_f32_16x16x32_bf16 v[80:83], v[220:223], a[144:147], v[80:83]\n" \
  "buffer_load_dwordx4 v[216:219], v10, %[rsW], s40 offen sc1\n" \
  "v_mfma_f32_16x16x32_bf16 v[84:87], v[220:223], a[148:151], v[84:87]\n" \
  "s_waitcnt lgkmcnt(3)\n" \
  "s_barrier\n" \
  "ds_read_b64_tr_b16 v[72:73], v44 offset:4096\n" \
  "ds_read_b64_tr_b16 v[74:75], v44 offset:4608\n" \
  "ds_read_b64_tr_b16 v[76:77], v45 offset:4096\n" \
  "v_mfma_f32_16x16x32_bf16 v[88:91], v[220:223], a[152:155], v[88:91]\n" \
  "s_waitcnt vmcnt(16)\n" \
  "v_mov_b32 v108, v24\n" \
  "ds_read_b32 v25, v108 offset:4\n" \
  "ds_read_b32 v21, v108\n" \
  "v_mfma_f32_16x16x32_bf16 v[92:95], v[220:223], a[156:159], v[92:95]\n" \
  "v_xor_b32 v108, 128, v24\n" \
  "ds_read_b32 v26, v108 offset:4\n" \
  "ds_read_b32 v22, v108\n" \
  "ds_read_b64_tr_b16 v[78:79], v45 offset:4608\n" \
  "v_mfma_f32_16x16x32_bf16 v[80:83], v[224:227], a[160:163], v[80:83]\n" \
  "buffer_load_dwordx4 v[220:223], v10, %[rsW], s40 offen offset:1024 sc1\n" \
  "v_mfma_f32_16x16x32_bf16 v[84:87], v[224:227], a[164:167], v[84:87]\n" \
  "v_xor_b32 v108, 256, v24\n" \
  "ds_read_b32 v27, v108 offset:4\n" \
  "ds_read_b32 v23, v108\n" \
  "v_mfma_f32_16x16x32_bf16 v[88:91], v[224:227], a[168:171], v[88:91]\n" \
  "s_waitcnt vmcnt(16)\n" \
  "v_xor_b32 v108, 384, v24\n" \
  "ds_read_b32 v28, v108 offset:4\n" \
  "ds_read_b32 v24, v108\n" \
  "v_mfma_f32_16x16x32_bf16 v[92:95], v[224:227], a[172:175], v[92:95]\n" \
  "s_waitcnt lgkmcnt(13)\n" \
  "ds_read_b128 v[30:33], v29\n" \
  "ds_read_b128 v[34:37], v29 offset:2304\n" \
  "v_mfma_f32_16x16x32_bf16 v[80:83], v[228:231], a[176:179], v[80:83]\n" \
  "buffer_load_dwordx4 v[224:227], v10, %[rsW], s40 offen offset:2048 sc1\n" \
  "v_mfma_f32_16x16x32_bf16 v[84:87], v[228:231], a[180:183], v[84:87]\n" \
  "v_mfma_f32_16x16x32_bf16 v[88:91], v[228:231], a[184:187], v[88:91]\n" \
  "s_waitcnt vmcnt(16)\n" \
  "v_mfma_f32_16x16x32_bf16 v[92:95], v[228:231], a[188:191], v[92:95]\n" \
  "v_mfma_f32_16x16x32_bf16 v[80:83], v[232:235], a[192:195], v[80:83]\n" \
  "buffer_load_dwordx4 v[228:231], v10, %[rsW], s40 offen offset:3072 sc1\n" \
  "v_mfma_f32_16x16x32_bf16 v[84:87], v[232:235], a[196:199], v[84:87]\n" \
  "v_mfma_f32_16x16x32_bf16 v[88:91], v[232:235], a[200:203], v[88:91]\n" \
  "s_waitcnt vmcnt(16)\n" \
  "v_mfma_f32_16x16x32_bf16 v[92:95], v[232:235], a[204:207], v[92:95]\n" \
  "v_mfma_f32_16x16x32_bf16 v[80:83], v[236:239], a[208:211], v[80:83]\n" \
  "buffer_load_dwordx4 v[232:235], v11, %[rsW], s40 offen sc1\n" \
  "v_mfma_f32_16x16x32_bf16 v[84:87], v[236:239], a[212:215], v[84:87]\n" \
  "v_mfma_f32_16x16x32_bf16 v[88:91], v[236:239], a[216:219], v[88:91]\n" \
  "s_waitcnt vmcnt(16)\n" \
  "v_mfma_f32_16x16x32_bf16 v[92:95], v[236:239], a[220:223], v[92:95]\n" \
  "v_mfma_f32_16x16x32_bf16 v[80:83], v[240:243], a[224:227], v[80:83]\n" \
  "buffer_load_dwordx4 v[236:239], v11, %[rsW], s40 offen offset:1024 sc1\n" \
  "v_mfma_f32_16x16x32_bf16 v[84:87], v[240:243], a[228:231], v[84:87]\n" \
  "v_mfma_f32_16x16x32_bf16 v[88:91], v[240:243], a[232:235], v[88:91]\n" \
  "s_waitcnt vmcnt(16)\n" \
  "v_mfma_f32_16x16x32_bf16 v[92:95], v[240:243], a[236:239], v[92:95]\n" \
  "v_mfma_f32_16x16x32_bf16 v[80:83], v[244:247], a[240:243], v[80:83]\n" \
  "buffer_load_dwordx4 v[240:243], v11, %[rsW], s40 offen offset:2048 sc1\n" \
  "v_mfma_f32_16x16x32_bf16 v[84:87], v[244:247], a[244:247], v[84:87]\n" \
  "v_mfma_f32_16x16x32_bf16 v[88:91], v[244:247], a[248:251], v[88:91]\n" \
  "s_waitcnt vmcnt(16)\n" \
  "v_mfma_f32_16x16x32_bf16 v[92:95], v[244:247], a[252:255], v[92:95]\n" \
  "v_mfma_f32_16x16x32_bf16 v[80:83], v[248:251], v[48:51], v[80:83]\n" \
  "buffer_load_dwordx4 v[244:247], v11, %[rsW], s40 offen offset:3072 sc1\n" \
  "v_mfma_f32_16x16x32_bf16 v[84:87], v[248:251], v[52:55], v[84:87]\n" \
  "v_mfma_f32_16x16x32_bf16 v[88:91], v[248:251], v[56:59], v[88:91]\n" \
  "s_waitcnt vmcnt(16)\n" \
  "v_mfma_f32_16x16x32_bf16 v[92:95], v[248:251], v[60:63], v[92:95]\n" \
  "v_mfma_f32_16x16x32_bf16 v[80:83], v[252:255], v[64:67], v[80:83]\n" \
  "buffer_load_dwordx4 v[248:251], v12, %[rsW], s40 offen sc1\n" \
  "s_waitcnt lgkmcnt(14)\n" \
  "v_mfma_f32_16x16x32_bf16 v[84:87], v[252:255], v[68:71], v[84:87]\n" \
  "s_waitcnt lgkmcnt(12)\n" \
  "v_mfma_f32_16x16x32_bf16 v[88:91], v[252:255], v[72:75], v[88:91]\n" \
  "s_waitcnt vmcnt(16)\n" \
  "s_waitcnt lgkmcnt(6)\n" \
  "v_mfma_f32_16x16x32_bf16 v[92:95], v[252:255], v[76:79], v[92:95]\n" \
  "s_mov_b32 s41, 4\n" \
  "L_g1_%=:\n" \
  "v_mfma_f32_16x16x32_bf16 v[96:99], v[184:187], a[0:3], 0\n" \
  "buffer_load_dwordx4 v[252:255], v12, %[rsW], s40 offen offset:1024 sc1\n" \
  "v_mfma_f32_16x16x32_bf16 v[100:103], v[184:187], a[4:7], 0\n" \
  "v_mfma_f32_16x16x32_bf16 v[104:107], v[184:187], a[8:11], 0\n" \
  "s_waitcnt vmcnt(16)\n" \
  "v_mfma_f32_16x16x32_bf16 v[108:111], v[184:187], a[12:15], 0\n" \
  "v_mfma_f32_16x16x32_bf16 v[96:99], v[188:191], a[16:19], v[96:99]\n" \
  "s_add_u32 s40, s40, 18432\n" \
  "buffer_load_dwordx4 v[184:187], v8, %[rsW], s40 offen sc1\n" \
  "v_mfma_f32_16x16x32_bf16 v[100:103], v[188:191], a[20:23], v[100:103]\n" \
  "s_waitcnt lgkmcnt(0)\n" \
  "v_fma_f32 v38, -v25, v30, v34\n" \
  "v_fma_f32 v39, -v25, v31, v35\n" \
  "v_mfma_f32_16x16x32_bf16 v[104:107], v[188:191], a[24:27], v[104:107]\n" \
  "s_waitcnt vmcnt(16)\n" \
  "v_fma_f32 v40, -v25, v32, v36\n" \
  "v_mfma_f32_16x16x32_bf16 v[108:111], v[188:191], a[28:31], v[108:111]\n" \
  "v_fma_f32 v41, -v25, v33, v37\n" \
  "v_fma_f32 v80, v21, v80, v38\n" \
  "v_mfma_f32_16x16x32_bf16 v[96:99], v[192:195], a[32:35], v[96:99]\n" \
  "buffer_load_dwordx4 v[188:191], v8, %[rsW], s40 offen offset:1024 sc1\n" \
  "v_mfma_f32_16x16x32_bf16 v[100:103], v[192:195], a[36:39], v[100:103]\n" \
  "v_fma_f32 v81, v21, v81, v39\n" \
  "v_fma_f32 v82, v21, v82, v40\n" \
  "v_mfma_f32_16x16x32_bf16 v[104:107], v[192:195], a[40:43], v[104:107]\n" \
  "s_waitcnt vmcnt(16)\n" \
  "v_fma_f32 v83, v21, v83, v41\n" \
  "v_mfma_f32_16x16x32_bf16 v[108:111], v[192:195], a[44:47], v[108:111]\n" \
  "v_max_f32 v80, 0, v80\n" \
  "v_max_f32 v81, 0, v81\n" \
  "v_mfma_f32_16x16x32_bf16 v[96:99], v[196:199], a[48:51], v[96:99]\n" \
  "buffer_load_dwordx4 v[192:195], v8, %[rsW], s40 offen offset:2048 sc1\n" \
  "v_mfma_f32_16x16x32_bf16 v[100:103], v[196:199], a[52:55], v[100:103]\n" \
  "v_max_f32 v82, 0, v82\n" \
  "v_max_f32 v83, 0, v83\n" \
  "v_mfma_f32_16x16x32_bf16 v[104:107], v[196:199], a[56:59], v[104:107]\n" \
  "s_waitcnt vmcnt(16)\n" \
  "v_mul_f32 v80, v80, v80\n" \
  "v_mfma_f32_16x16x32_bf16 v[108:111], v[196:199], a[60:63], v[108:111]\n" \
  "v_mul_f32 v81, v81, v81\n" \
  "v_mul_f32 v82, v82, v82\n" \
  "v_mfma_f32_16x16x32_bf16 v[96:99], v[200:203], a[64:67], v[96:99]\n" \
  "buffer_load_dwordx4 v[196:199], v8, %[rsW], s40 offen offset:3072 sc1\n" \
  "v_mfma_f32_16x16x32_bf16 v[100:103], v[200:203], a[68:71], v[100:103]\n" \
  "v_mul_f32 v83, v83, v83\n" \
  "v_cvt_pk_bf16_f32 v42, v80, v81\n" \
  "v_mfma_f32_16x16x32_bf16 v[104:107], v[200:203], a[72:75], v[104:107]\n" \
  "s_waitcnt vmcnt(16)\n" \
  "v_cvt_pk_bf16_f32 v43, v82, v83\n" \
  "ds_write_b64 v17, v[42:43]\n" \
  "v_mfma_f32_16x16x32_bf16 v[108:111], v[200:203], a[76:79], v[108:111]\n" \
  "v_fma_f32 v38, -v26, v30, v34\n" \
  "v_fma_f32 v39, -v26, v31, v35\n" \
  "v_mfma_f32_16x16x32_bf16 v[96:99], v[204:207], a[80:83], v[96:99]\n" \
  "buffer_load_dwordx4 v[200:203], v9, %[rsW], s40 offen sc1\n" \
  "v_mfma_f32_16x16x32_bf16 v[100:103], v[204:207], a[84:87], v[100:103]\n" \
  "v_fma_f32 v40, -v26, v32, v36\n" \
  "v_fma_f32 v41, -v26, v33, v37\n" \
  "v_mfma_f32_16x16x32_bf16 v[104:107], v[204:207], a[88:91], v[104:107]\n" \
  "s_waitcnt vmcnt(16)\n" \
  "v_fma_f32 v84, v22, v84, v38\n" \
  "v_mfma_f32_16x16x32_bf16 v[108:111], v[204:207], a[92:95], v[108:111]\n" \
  "v_fma_f32 v85, v22, v85, v39\n" \
  "v_fma_f32 v86, v22, v86, v40\n" \
  "v_mfma_f32_16x16x32_bf16 v[96:99], v[208:211], a[96:99], v[96:99]\n" \
  "buffer_load_dwordx4 v[204:207], v9, %[rsW], s40 offen offset:1024 sc1\n" \
  "v_mfma_f32_16x16x32_bf16 v[100:103], v[208:211], a[100:103], v[100:103]\n" \
  "v_fma_f32 v87, v22, v87, v41\n" \
  "v_max_f32 v84, 0, v84\n" \
  "v_mfma_f32_16x16x32_bf16 v[104:107], v[208:211], a[104:107], v[104:107]\n" \
  "s_waitcnt vmcnt(16)\n" \
  "v_max_f32 v85, 0, v85\n" \
  "v_mfma_f32_16x16x32_bf16 v[108:111], v[208:211], a[108:111], v[108:111]\n" \
  "v_max_f32 v86, 0, v86\n" \
  "v_max_f32 v87, 0, v87\n" \
  "v_mfma_f32_16x16x32_bf16 v[96:99], v[212:215], a[112:115], v[96:99]\n" \
  "buffer_load_dwordx4 v[208:211], v9, %[rsW], s40 offen offset:2048 sc1\n" \
  "v_mfma_f32_16x16x32_bf16 v[100:103], v[212:215], a[116:119], v[100:103]\n" \
  "v_mul_f32 v84, v84, v84\n" \
  "v_mul_f32 v85, v85, v85\n" \
  "v_mfma_f32_16x16x32_bf16 v[104:107], v[212:215], a[120:123], v[104:107]\n" \
  "s_waitcnt vmcnt(16)\n" \
  "v_mul_f32 v86, v86, v86\n" \
  "v_mfma_f32_16x16x32_bf16 v[108:111], v[212:215], a[124:127], v[108:111]\n" \
  "v_mul_f32 v87, v87, v87\n" \
  "v_cvt_pk_bf16_f32 v44, v84, v85\n" \
  "v_mfma_f32_16x16x32_bf16 v[96:99], v[216:219], a[128:131], v[96:99]\n" \
  "buffer_load_dwordx4 v[212:215], v9, %[rsW], s40 offen offset:3072 sc1\n" \
  "v_mfma_f32_16x16x32_bf16 v[100:103], v[216:219], a[132:135], v[100:103]\n" \
  "v_cvt_pk_bf16_f32 v45, v86, v87\n" \
  "ds_write_b64 v18, v[44:45]\n" \
  "v_fma_f32 v38, -v27, v30, v34\n" \
  "v_mfma_f32_16x16x32_bf16 v[104:107], v[216:219], a[136:139], v[104:107]\n" \
  "s_waitcnt vmcnt(16)\n" \
  "v_fma_f32 v39, -v27, v31, v35\n" \
  "v_mfma_f32_16x16x32_bf16 v[108:111], v[216:219], a[140:143], v[108:111]\n" \
  "v_fma_f32 v40, -v27, v32, v36\n" \
  "v_fma_f32 v41, -v27, v33, v37\n" \
  "v_mfma_f32_16x16x32_bf16 v[96:99], v[220:223], a[144:147], v[96:99]\n" \
  "buffer_load_dwordx4 v[216:219], v10, %[rsW], s40 offen sc1\n" \
  "v_mfma_f32_16x16x32_bf16 v[100:103], v[220:223], a[148:151], v[100:103]\n" \
  "v_fma_f32 v88, v23, v88, v38\n" \
  "v_fma_f32 v89, v23, v89, v39\n" \
  "v_mfma_f32_16x16x32_bf16 v[104:107], v[220:223], a[152:155], v[104:107]\n" \
  "s_waitcnt vmcnt(16)\n" \
  "v_fma_f32 v90, v23, v90, v40\n" \
  "v_mfma_f32_16x16x32_bf16 v[108:111], v[220:223], a[156:159], v[108:111]\n" \
  "v_fma_f32 v91, v23, v91, v41\n" \
  "v_max_f32 v88, 0, v88\n" \
  "v_mfma_f32_16x16x32_bf16 v[96:99], v[224:227], a[160:163], v[96:99]\n" \
  "buffer_load_dwordx4 v[220:223], v10, %[rsW], s40 offen offset:1024 sc1\n" \
  "v_mfma_f32_16x16x32_bf16 v[100:103], v[224:227], a[164:167], v[100:103]\n" \
  "v_max_f32 v89, 0, v89\n" \
  "v_max_f32 v90, 0, v90\n" \
  "v_mfma_f32_16x16x32_bf16 v[104:107], v[224:227], a[168:171], v[104:107]\n" \
  "s_waitcnt vmcnt(16)\n" \
  "v_max_f32 v91, 0, v91\n" \
  "v_mfma_f32_16x16x32_bf16 v[108:111], v[224:227], a[172:175], v[108:111]\n" \
  "v_mul_f32 v88, v88, v88\n" \
  "v_mul_f32 v89, v89, v89\n" \
  "v_mfma_f32_16x16x32_bf16 v[96:99], v[228:231], a[176:179], v[96:99]\n" \
  "buffer_load_dwordx4 v[224:227], v10, %[rsW], s40 offen offset:2048 sc1\n" \
  "v_mfma_f32_16x16x32_bf16 v[100:103], v[228:231], a[180:183], v[100:103]\n" \
  "v_mul_f32 v90, v90, v90\n" \
  "v_mul_f32 v91, v91, v91\n" \
  "v_mfma_f32_16x16x32_bf16 v[104:107], v[228:231], a[184:187], v[104:107]\n" \
  "s_waitcnt vmcnt(16)\n" \
  "v_cvt_pk_bf16_f32 v42, v88, v89\n" \
  "v_mfma_f32_16x16x32_bf16 v[108:111], v[228:231], a[188:191], v[108:111]\n" \
  "v_cvt_pk_bf16_f32 v43, v90, v91\n" \
  "ds_write_b64 v19, v[42:43]\n" \
  "v_fma_f32 v38, -v28, v30, v34\n" \
  "v_mfma_f32_16x16x32_bf16 v[96:99], v[232:235], a[192:195], v[96:99]\n" \
  "buffer_load_dwordx4 v[228:231], v10, %[rsW], s40 offen offset:3072 sc1\n" \
  "v_mfma_f32_16x16x32_bf16 v[100:103], v[232:235], a[196:199], v[100:103]\n" \
  "v_fma_f32 v39, -v28, v31, v35\n" \
  "v_fma_f32 v40, -v28, v32, v36\n" \
  "v_mfma_f32_16x16x32_bf16 v[104:107], v[232:235], a[200:203], v[104:107]\n" \
  "s_waitcnt vmcnt(16)\n" \
  "v_fma_f32 v41, -v28, v33, v37\n" \
  "v_mfma_f32_16x16x32_bf16 v[108:111], v[232:235], a[204:207], v[108:111]\n" \
  "v_fma_f32 v92, v24, v92, v38\n" \
  "v_fma_f32 v93, v24, v93, v39\n" \
  "v_mfma_f32_16x16x32_bf16 v[96:99], v[236:239], a[208:211], v[96:99]\n" \
  "buffer_load_dwordx4 v[232:235], v11, %[rsW], s40 offen sc1\n" \
  "v_mfma_f32_16x16x32_bf16 v[100:103], v[236:239], a[212:215], v[100:103]\n" \
  "v_fma_f32 v94, v24, v94, v40\n" \
  "v_fma_f32 v95, v24, v95, v41\n" \
  "v_mfma_f32_16x16x32_bf16 v[104:107], v[236:239], a[216:219], v[104:107]\n" \
  "s_waitcnt vmcnt(16)\n" \
  "v_max_f32 v92, 0, v92\n" \
  "v_mfma_f32_16x16x32_bf16 v[108:111], v[236:239], a[220:223], v[108:111]\n" \
  "v_max_f32 v93, 0, v93\n" \
  "v_max_f32 v94, 0, v94\n" \
  "v_mfma_f32_16x16x32_bf16 v[96:99], v[240:243], a[224:227], v[96:99]\n" \
  "buffer_load_dwordx4 v[236:239], v11, %[rsW], s40 offen offset:1024 sc1\n" \
  "v_mfma_f32_16x16x32_bf16 v[100:103], v[240:243], a[228:231], v[100:103]\n" \
  "v_max_f32 v95, 0, v95\n" \
  "v_mul_f32 v92, v92, v92\n" \
  "v_mfma_f32_16x16x32_bf16 v[104:107], v[240:243], a[232:235], v[104:107]\n" \
  "s_waitcnt vmcnt(16)\n" \
  "v_mul_f32 v93, v93, v93\n" \
  "v_mfma_f32_16x16x32_bf16 v[108:111], v[240:243], a[236:239], v[108:111]\n" \
  "v_mul_f32 v94, v94, v94\n" \
  "v_mul_f32 v95, v95, v95\n" \
  "v_mfma_f32_16x16x32_bf16 v[96:99], v[244:247], a[240:243], v[96:99]\n" \
  "buffer_load_dwordx4 v[240:243], v11, %[rsW], s40 offen offset:2048 sc1\n" \
  "v_mfma_f32_16x16x32_bf16 v[100:103], v[244:247], a[244:247], v[100:103]\n" \
  "v_cvt_pk_bf16_f32 v44, v92, v93\n" \
  "v_mfma_f32_16x16x32_bf16 v[104:107], v[244:247], a[248:251], v[104:107]\n" \
  "s_waitcnt vmcnt(16)\n" \
  "v_cvt_pk_bf16_f32 v45, v94, v95\n" \
  "ds_write_b64 v20, v[44:45]\n" \
  "v_mfma_f32_16x16x32_bf16 v[108:111], v[244:247], a[252:255], v[108:111]\n" \
  "v_mfma_f32_16x16x32_bf16 v[96:99], v[248:251], v[48:51], v[96:99]\n" \
  "buffer_load_dwordx4 v[244:247], v11, %[rsW], s40 offen offset:3072 sc1\n" \
  "v_mfma_f32_16x16x32_bf16 v[100:103], v[248:251], v[52:55], v[100:103]\n" \
  "v_mfma_f32_16x16x32_bf16 v[104:107], v[248:251], v[56:59], v[104:107]\n" \
  "s_waitcnt vmcnt(16)\n" \
  "v_mfma_f32_16x16x32_bf16 v[108:111], v[248:251], v[60:63], v[108:111]\n" \
  "v_mfma_f32_16x16x32_bf16 v[96:99], v[252:255], v[64:67], v[96:99]\n" \
  "buffer_load_dwordx4 v[248:251], v12, %[rsW], s40 offen sc1\n" \
  "v_mfma_f32_16x16x32_bf16 v[100:103], v[252:255], v[68:71], v[100:103]\n" \
  "ds_read_b128 v[30:33], v29 offset:64\n" \
  "ds_read_b128 v[34:37], v29 offset:2368\n" \
  "v_mfma_f32_16x16x32_bf16 v[104:107], v[252:255], v[72:75], v[104:107]\n" \
  "s_waitcnt vmcnt(16)\n" \
  "v_mfma_f32_16x16x32_bf16 v[108:111], v[252:255], v[76:79], v[108:111]\n" \
  "v_mfma_f32_16x16x32_bf16 v[80:83], v[184:187], a[0:3], 0\n" \
  "buffer_load_dwordx4 v[252:255], v12, %[rsW], s40 offen offset:1024 sc1\n" \
  "v_mfma_f32_16x16x32_bf16 v[84:87], v[184:187], a[4:7], 0\n" \
  "v_mfma_f32_16x16x32_bf16 v[88:91], v[184:187], a[8:11], 0\n" \
  "s_waitcnt vmcnt(16)\n" \
  "v_mfma_f32_16x16x32_bf16 v[92:95], v[184:187], a[12:15], 0\n" \
  "v_mfma_f32_16x16x32_bf16 v[80:83], v[188:191], a[16:19], v[80:83]\n" \
  "s_add_u32 s40, s40, 18432\n" \
  "buffer_load_dwordx4 v[184:187], v8, %[rsW], s40 offen sc1\n" \
  "v_mfma_f32_16x16x32_bf16 v[84:87], v[188:191], a[20:23], v[84:87]\n" \
  "s_waitcnt lgkmcnt(0)\n" \
  "v_fma_f32 v38, -v25, v30, v34\n" \
  "v_fma_f32 v39, -v25, v31, v35\n" \
  "v_mfma_f32_16x16x32_bf16 v[88:91], v[188:191], a[24:27], v[88:91]\n" \
  "s_waitcnt vmcnt(16)\n" \
  "v_fma_f32 v40, -v25, v32, v36\n" \
  "v_mfma_f32_16x16x32_bf16 v[92:95], v[188:191], a[28:31], v[92:95]\n" \
  "v_fma_f32 v41, -v25, v33, v37\n" \
  "v_fma_f32 v96, v21, v96, v38\n" \
  "v_mfma_f32_16x16x32_bf16 v[80:83], v[192:195], a[32:35], v[80:83]\n" \
  "buffer_load_dwordx4 v[188:191], v8, %[rsW], s40 offen offset:1024 sc1\n" \
  "v_mfma_f32_16x16x32_bf16 v[84:87], v[192:195], a[36:39], v[84:87]\n" \
  "v_fma_f32 v97, v21, v97, v39\n" \
  "v_fma_f32 v98, v21, v98, v40\n" \
  "v_mfma_f32_16x16x32_bf16 v[88:91], v[192:195], a[40:43], v[88:91]\n" \
  "s_waitcnt vmcnt(16)\n" \
  "v_fma_f32 v99, v21, v99, v41\n" \
  "v_mfma_f32_16x16x32_bf16 v[92:95], v[192:195], a[44:47], v[92:95]\n" \
  "v_max_f32 v96, 0, v96\n" \
  "v_max_f32 v97, 0, v97\n" \
  "v_mfma_f32_16x16x32_bf16 v[80:83], v[196:199], a[48:51], v[80:83]\n" \
  "buffer_load_dwordx4 v[192:195], v8, %[rsW], s40 offen offset:2048 sc1\n" \
  "v_mfma_f32_16x16x32_bf16 v[84:87], v[196:199], a[52:55], v[84:87]\n" \
  "v_max_f32 v98, 0, v98\n" \
  "v_max_f32 v99, 0, v99\n" \
  "v_mfma_f32_16x16x32_bf16 v[88:91], v[196:199], a[56:59], v[88:91]\n" \
  "s_waitcnt vmcnt(16)\n" \
  "v_mul_f32 v96, v96, v96\n" \
  "v_mfma_f32_16x16x32_bf16 v[92:95], v[196:199], a[60:63], v[92:95]\n" \
  "v_mul_f32 v97, v97, v97\n" \
  "v_mul_f32 v98, v98, v98\n" \
  "v_mfma_f32_16x16x32_bf16 v[80:83], v[200:203], a[64:67], v[80:83]\n" \
  "buffer_load_dwordx4 v[196:199], v8, %[rsW], s40 offen offset:3072 sc1\n" \
  "v_mfma_f32_16x16x32_bf16 v[84:87], v[200:203], a[68:71], v[84:87]\n" \
  "v_mul_f32 v99, v99, v99\n" \
  "v_cvt_pk_bf16_f32 v42, v96, v97\n" \
  "v_mfma_f32_16x16x32_bf16 v[88:91], v[200:203], a[72:75], v[88:91]\n" \
  "s_waitcnt vmcnt(16)\n" \
  "v_cvt_pk_bf16_f32 v43, v98, v99\n" \
  "ds_write_b64 v17, v[42:43] offset:8\n" \
  "v_mfma_f32_16x16x32_bf16 v[92:95], v[200:203], a[76:79], v[92:95]\n" \
  "v_fma_f32 v38, -v26, v30, v34\n" \
  "v_fma_f32 v39, -v26, v31, v35\n" \
  "v_mfma_f32_16x16x32_bf16 v[80:83], v[204:207], a[80:83], v[80:83]\n" \
  "buffer_load_dwordx4 v[200:203], v9, %[rsW], s40 offen sc1\n" \
  "v_mfma_f32_16x16x32_bf16 v[84:87], v[204:207], a[84:87], v[84:87]\n" \
  "v_fma_f32 v40, -v26, v32, v36\n" \
  "v_fma_f32 v41, -v26, v33, v37\n" \
  "v_mfma_f32_16x16x32_bf16 v[88:91], v[204:207], a[88:91], v[88:91]\n" \
  "s_waitcnt vmcnt(16)\n" \
  "v_fma_f32 v100, v22, v100, v38\n" \
  "v_mfma_f32_16x16x32_bf16 v[92:95], v[204:207], a[92:95], v[92:95]\n" \
  "v_fma_f32 v101, v22, v101, v39\n" \
  "v_fma_f32 v102, v22, v102, v40\n" \
  "v_mfma_f32_16x16x32_bf16 v[80:83], v[208:211], a[96:99], v[80:83]\n" \
  "buffer_load_dwordx4 v[204:207], v9, %[rsW], s40 offen offset:1024 sc1\n" \
  "v_mfma_f32_16x16x32_bf16 v[84:87], v[208:211], a[100:103], v[84:87]\n" \
  "v_fma_f32 v103, v22, v103, v41\n" \
  "v_max_f32 v100, 0, v100\n" \
  "v_mfma_f32_16x16x32_bf16 v[88:91], v[208:211], a[104:107], v[88:91]\n" \
  "s_waitcnt vmcnt(16)\n" \
  "v_max_f32 v101, 0, v101\n" \
  "v_mfma_f32_16x16x32_bf16 v[92:95], v[208:211], a[108:111], v[92:95]\n" \
  "v_max_f32 v102, 0, v102\n" \
  "v_max_f32 v103, 0, v103\n" \
  "v_mfma_f32_16x16x32_bf16 v[80:83], v[212:215], a[112:115], v[80:83]\n" \
  "buffer_load_dwordx4 v[208:211], v9, %[rsW], s40 offen offset:2048 sc1\n" \
  "v_mfma_f32_16x16x32_bf16 v[84:87], v[212:215], a[116:119], v[84:87]\n" \
  "v_mul_f32 v100, v100, v100\n" \
  "v_mul_f32 v101, v101, v101\n" \
  "v_mfma_f32_16x16x32_bf16 v[88:91], v[212:215], a[120:123], v[88:91]\n" \
  "s_waitcnt vmcnt(16)\n" \
  "v_mul_f32 v102, v102, v102\n" \
  "v_mfma_f32_16x16x32_bf16 v[92:95], v[212:215], a[124:127], v[92:95]\n" \
  "v_mul_f32 v103, v103, v103\n" \
  "v_cvt_pk_bf16_f32 v44, v100, v101\n" \
  "v_mfma_f32_16x16x32_bf16 v[80:83], v[216:219], a[128:131], v[80:83]\n" \
  "buffer_load_dwordx4 v[212:215], v9, %[rsW], s40 offen offset:3072 sc1\n" \
  "v_mfma_f32_16x16x32_bf16 v[84:87], v[216:219], a[132:135], v[84:87]\n" \
  "v_cvt_pk_bf16_f32 v45, v102, v103\n" \
  "ds_write_b64 v18, v[44:45] offset:8\n" \
  "v_fma_f32 v38, -v27, v30, v34\n" \
  "v_mfma_f32_16x16x32_bf16 v[88:91], v[216:219], a[136:139], v[88:91]\n" \
  "s_waitcnt vmcnt(16)\n" \
  "v_fma_f32 v39, -v27, v31, v35\n" \
  "v_mfma_f32_16x16x32_bf16 v[92:95], v[216:219], a[140:143], v[92:95]\n" \
  "v_fma_f32 v40, -v27, v32, v36\n" \
  "v_fma_f32 v41, -v27, v33, v37\n" \
  "v_mfma_f32_16x16x32_bf16 v[80:83], v[220:223], a[144:147], v[80:83]\n" \
  "buffer_load_dwordx4 v[216:219], v10, %[rsW], s40 offen sc1\n" \
  "v_mfma_f32_16x16x32_bf16 v[84:87], v[220:223], a[148:151], v[84:87]\n" \
  "v_fma_f32 v104, v23, v104, v38\n" \
  "v_fma_f32 v105, v23, v105, v39\n" \
  "v_mfma_f32_16x16x32_bf16 v[88:91], v[220:223], a[152:155], v[88:91]\n" \
  "s_waitcnt vmcnt(16)\n" \
  "v_fma_f32 v106, v23, v106, v40\n" \
  "v_mfma_f32_16x16x32_bf16 v[92:95], v[220:223], a[156:159], v[92:95]\n" \
  "v_fma_f32 v107, v23, v107, v41\n" \
  "v_max_f32 v104, 0, v104\n" \
  "v_mfma_f32_16x16x32_bf16 v[80:83], v[224:227], a[160:163], v[80:83]\n" \
  "buffer_load_dwordx4 v[220:223], v10, %[rsW], s40 offen offset:1024 sc1\n" \
  "v_mfma_f32_16x16x32_bf16 v[84:87], v[224:227], a[164:167], v[84:87]\n" \
  "v_max_f32 v105, 0, v105\n" \
  "v_max_f32 v106, 0, v106\n" \
  "v_mfma_f32_16x16x32_bf16 v[88:91], v[224:227], a[168:171], v[88:91]\n" \
  "s_waitcnt vmcnt(16)\n" \
  "v_max_f32 v107, 0, v107\n" \
  "v_mfma_f32_16x16x32_bf16 v[92:95], v[224:227], a[172:175], v[92:95]\n" \
  "v_mul_f32 v104, v104, v104\n" \
  "v_mul_f32 v105, v105, v105\n" \
  "v_mfma_f32_16x16x32_bf16 v[80:83], v[228:231], a[176:179], v[80:83]\n" \
  "buffer_load_dwordx4 v[224:227], v10, %[rsW], s40 offen offset:2048 sc1\n" \
  "v_mfma_f32_16x16x32_bf16 v[84:87], v[228:231], a[180:183], v[84:87]\n" \
  "v_mul_f32 v106, v106, v106\n" \
  "v_mul_f32 v107, v107, v107\n" \
  "v_mfma_f32_16x16x32_bf16 v[88:91], v[228:231], a[184:187], v[88:91]\n" \
  "s_waitcnt vmcnt(16)\n" \
  "v_cvt_pk_bf16_f32 v42, v104, v105\n" \
  "v_mfma_f32_16x16x32_bf16 v[92:95], v[228:231], a[188:191], v[92:95]\n" \
  "v_cvt_pk_bf16_f32 v43, v106, v107\n" \
  "ds_write_b64 v19, v[42:43] offset:8\n" \
  "v_fma_f32 v38, -v28, v30, v34\n" \
  "v_mfma_f32_16x16x32_bf16 v[80:83], v[232:235], a[192:195], v[80:83]\n" \
  "buffer_load_dwordx4 v[228:231], v10, %[rsW], s40 offen offset:3072 sc1\n" \
  "v_mfma_f32_16x16x32_bf16 v[84:87], v[232:235], a[196:199], v[84:87]\n" \
  "v_fma_f32 v39, -v28, v31, v35\n" \
  "v_fma_f32 v40, -v28, v32, v36\n" \
  "v_mfma_f32_16x16x32_bf16 v[88:91], v[232:235], a[200:203], v[88:91]\n" \
  "s_waitcnt vmcnt(16)\n" \
  "v_fma_f32 v41, -v28, v33, v37\n" \
  "v_mfma_f32_16x16x32_bf16 v[92:95], v[232:235], a[204:207], v[92:95]\n" \
  "v_fma_f32 v108, v24, v108, v38\n" \
  "v_fma_f32 v109, v24, v109, v39\n" \
  "v_mfma_f32_16x16x32_bf16 v[80:83], v[236:239], a[208:211], v[80:83]\n" \
  "buffer_load_dwordx4 v[232:235], v11, %[rsW], s40 offen sc1\n" \
  "v_mfma_f32_16x16x32_bf16 v[84:87], v[236:239], a[212:215], v[84:87]\n" \
  "v_fma_f32 v110, v24, v110, v40\n" \
  "v_fma_f32 v111, v24, v111, v41\n" \
  "v_mfma_f32_16x16x32_bf16 v[88:91], v[236:239], a[216:219], v[88:91]\n" \
  "s_waitcnt vmcnt(16)\n" \
  "v_max_f32 v108, 0, v108\n" \
  "v_mfma_f32_16x16x32_bf16 v[92:95], v[236:239], a[220:223], v[92:95]\n" \
  "v_max_f32 v109, 0, v109\n" \
  "v_max_f32 v110, 0, v110\n" \
  "v_mfma_f32_16x16x32_bf16 v[80:83], v[240:243], a[224:227], v[80:83]\n" \
  "buffer_load_dwordx4 v[236:239], v11, %[rsW], s40 offen offset:1024 sc1\n" \
  "v_mfma_f32_16x16x32_bf16 v[84:87], v[240:243], a[228:231], v[84:87]\n" \
  "v_max_f32 v111, 0, v111\n" \
  "v_mul_f32 v108, v108, v108\n" \
  "v_mfma_f32_16x16x32_bf16 v[88:91], v[240:243], a[232:235], v[88:91]\n" \
  "s_waitcnt vmcnt(16)\n" \
  "v_mul_f32 v109, v109, v109\n" \
  "v_mfma_f32_16x16x32_bf16 v[92:95], v[240:243], a[236:239], v[92:95]\n" \
  "v_mul_f32 v110, v110, v110\n" \
  "v_mul_f32 v111, v111, v111\n" \
  "v_mfma_f32_16x16x32_bf16 v[80:83], v[244:247], a[240:243], v[80:83]\n" \
  "buffer_load_dwordx4 v[240:243], v11, %[rsW], s40 offen offset:2048 sc1\n" \
  "v_mfma_f32_16x16x32_bf16 v[84:87], v[244:247], a[244:247], v[84:87]\n" \
  "v_cvt_pk_bf16_f32 v44, v108, v109\n" \
  "v_mfma_f32_16x16x32_bf16 v[88:91], v[244:247], a[248:251], v[88:91]\n" \
  "s_waitcnt vmcnt(16)\n" \
  "v_cvt_pk_bf16_f32 v45, v110, v111\n" \
  "ds_write_b64 v20, v[44:45] offset:8\n" \
  "v_mfma_f32_16x16x32_bf16 v[92:95], v[244:247], a[252:255], v[92:95]\n" \
  "v_mfma_f32_16x16x32_bf16 v[80:83], v[248:251], v[48:51], v[80:83]\n" \
  "buffer_load_dwordx4 v[244:247], v11, %[rsW], s40 offen offset:3072 sc1\n" \
  "v_mfma_f32_16x16x32_bf16 v[84:87], v[248:251], v[52:55], v[84:87]\n" \
  "v_mfma_f32_16x16x32_bf16 v[88:91], v[248:251], v[56:59], v[88:91]\n" \
  "s_waitcnt vmcnt(16)\n" \
  "v_mfma_f32_16x16x32_bf16 v[92:95], v[248:251], v[60:63], v[92:95]\n" \
  "v_mfma_f32_16x16x32_bf16 v[80:83], v[252:255], v[64:67], v[80:83]\n" \
  "buffer_load_dwordx4 v[248:251], v12, %[rsW], s40 offen sc1\n" \
  "v_mfma_f32_16x16x32_bf16 v[84:87], v[252:255], v[68:71], v[84:87]\n" \
  "ds_read_b128 v[30:33], v29 offset:128\n" \
  "ds_read_b128 v[34:37], v29 offset:2432\n" \
  "v_mfma_f32_16x16x32_bf16 v[88:91], v[252:255], v[72:75], v[88:91]\n" \
  "s_waitcnt vmcnt(16)\n" \
  "v_add_u32 v29, 128, v29\n" \
  "v_mfma_f32_16x16x32_bf16 v[92:95], v[252:255], v[76:79], v[92:95]\n" \
  "v_add_u32 v17, 4096, v17\n" \
  "v_add_u32 v18, 4096, v18\n" \
  "v_add_u32 v19, 4096, v19\n" \
  "v_add_u32 v20, 4096, v20\n" \
  "s_sub_u32 s41, s41, 1\n" \
  "s_cmp_lg_u32 s41, 0\n" \
  "s_cbranch_scc1 L_g1_%=\n" \
  "s_waitcnt lgkmcnt(2)\n" \
  "s_barrier\n" \
  "v_add_u32 v46, %[ldsb], v8\n" \
  "v_add_u32 v46, 73728, v46\n" \
  "v_add_u32 v47, 65536, v46\n" \
  "s_lshr_b32 s43, %[w], 1\n" \
  "s_add_u32 s43, s43, 16\n" \
  "s_lshl_b32 s43, s43, 12\n" \
  "s_lshl_b32 s44, %[w], 10\n" \
  "s_add_u32 s43, s43, s44\n" \
  "s_and_b32 s44, %[w], 1\n" \
  "s_lshl_b32 s44, s44, 3\n" \
  "s_add_u32 s43, s43, s44\n" \
  "v_add_u32 v17, s43, v46\n" \
  "v_xor_b32 v18, 1024, v17\n" \
  "v_xor_b32 v19, 2048, v17\n" \
  "v_xor_b32 v20, 3072, v17\n" \
  "ds_read_b128 a[0:3], v46\n" \
  "ds_read_b128 a[4:7], v46 offset:1024\n" \
  "ds_read_b128 a[8:11], v46 offset:2048\n" \
  "ds_read_b128 a[12:15], v46 offset:3072\n" \
  "ds_read_b128 a[16:19], v46 offset:4096\n" \
  "ds_read_b128 a[20:23], v46 offset:5120\n" \
  "ds_read_b128 a[24:27], v46 offset:6144\n" \
  "ds_read_b128 a[28:31], v46 offset:7168\n" \
  "ds_read_b128 a[32:35], v46 offset:8192\n" \
  "ds_read_b128 a[36:39], v46 offset:9216\n" \
  "ds_read_b128 a[40:43], v46 offset:10240\n" \
  "ds_read_b128 a[44:47], v46 offset:11264\n" \
  "s_waitcnt lgkmcnt(11)\n" \
  "v_mfma_f32_16x16x32_bf16 v[96:99], v[184:187], a[0:3], 0\n" \
  "buffer_load_dwordx4 v[252:255], v12, %[rsW], s40 offen offset:1024 sc1\n" \
  "ds_read_b128 a[48:51], v46 offset:12288\n" \
  "s_waitcnt lgkmcnt(11)\n" \
  "v_mfma_f32_16x16x32_bf16 v[100:103], v[184:187], a[4:7], 0\n" \
  "ds_read_b128 a[52:55], v46 offset:13312\n" \
  "s_waitcnt lgkmcnt(11)\n" \
  "v_mfma_f32_16x16x32_bf16 v[104:107], v[184:187], a[8:11], 0\n" \
  "s_waitcnt vmcnt(16)\n" \
  "ds_read_b128 a[56:59], v46 offset:14336\n" \
  "s_waitcnt lgkmcnt(11)\n" \
  "v_mfma_f32_16x16x32_bf16 v[108:111], v[184:187], a[12:15], 0\n" \
  "ds_read_b128 a[60:63], v46 offset:15360\n" \
  "s_waitcnt lgkmcnt(11)\n" \
  "v_mfma_f32_16x16x32_bf16 v[96:99], v[188:191], a[16:19], v[96:99]\n" \
  "s_add_u32 s40, s40, 18432\n" \
  "buffer_load_dwordx4 v[184:187], v8, %[rsW], s40 offen sc1\n" \
  "ds_read_b128 a[64:67], v46 offset:16384\n" \
  "s_waitcnt lgkmcnt(11)\n" \
  "v_mfma_f32_16x16x32_bf16 v[100:103], v[188:191], a[20:23], v[100:103]\n" \
  "v_fma_f32 v38, -v25, v30, v34\n" \
  "v_fma_f32 v39, -v25, v31, v35\n" \
  "ds_read_b128 a[68:71], v46 offset:17408\n" \
  "s_waitcnt lgkmcnt(11)\n" \
  "v_mfma_f32_16x16x32_bf16 v[104:107], v[188:191], a[24:27], v[104:107]\n" \
  "s_waitcnt vmcnt(16)\n" \
  "v_fma_f32 v40, -v25, v32, v36\n" \
  "v_fma_f32 v41, -v25, v33, v37\n" \
  "ds_read_b128 a[72:75], v46 offset:18432\n" \
  "s_waitcnt lgkmcnt(11)\n" \
  "v_mfma_f32_16x16x32_bf16 v[108:111], v[188:191], a[28:31], v[108:111]\n" \
  "v_fma_f32 v80, v21, v80, v38\n" \
  "v_fma_f32 v81, v21, v81, v39\n" \
  "ds_read_b128 a[76:79], v46 offset:19456\n" \
  "s_waitcnt lgkmcnt(11)\n" \
  "v_mfma_f32_16x16x32_bf16 v[96:99], v[192:195], a[32:35], v[96:99]\n" \
  "buffer_load_dwordx4 v[188:191], v8, %[rsW], s40 offen offset:1024 sc1\n" \
  "ds_read_b128 a[80:83], v46 offset:20480\n" \
  "s_waitcnt lgkmcnt(11)\n" \
  "v_mfma_f32_16x16x32_bf16 v[100:103], v[192:195], a[36:39], v[100:103]\n" \
  "v_fma_f32 v82, v21, v82, v40\n" \
  "v_fma_f32 v83, v21, v83, v41\n" \
  "ds_read_b128 a[84:87], v46 offset:21504\n" \
  "s_waitcnt lgkmcnt(11)\n" \
  "v_mfma_f32_16x16x32_bf16 v[104:107], v[192:195], a[40:43], v[104:107]\n" \
  "s_waitcnt vmcnt(16)\n" \
  "v_max_f32 v80, 0, v80\n" \
  "v_max_f32 v81, 0, v81\n" \
  "ds_read_b128 a[88:91], v46 offset:22528\n" \
  "s_waitcnt lgkmcnt(11)\n" \
  "v_mfma_f32_16x16x32_bf16 v[108:111], v[192:195], a[44:47], v[108:111]\n" \
  "v_max_f32 v82, 0, v82\n" \
  "v_max_f32 v83, 0, v83\n" \
  "ds_read_b128 a[92:95], v46 offset:23552\n" \
  "s_waitcnt lgkmcnt(11)\n" \
  "v_mfma_f32_16x16x32_bf16 v[96:99], v[196:199], a[48:51], v[96:99]\n" \
  "buffer_load_dwordx4 v[192:195], v8, %[rsW], s40 offen offset:2048 sc1\n" \
  "ds_read_b128 a[96:99], v46 offset:24576\n" \
  "s_waitcnt lgkmcnt(11)\n" \
  "v_mfma_f32_16x16x32_bf16 v[100:103], v[196:199], a[52:55], v[100:103]\n" \
  "v_mul_f32 v80, v80, v80\n" \
  "v_mul_f32 v81, v81, v81\n" \
  "ds_read_b128 a[100:103], v46 offset:25600\n" \
  "s_waitcnt lgkmcnt(11)\n" \
  "v_mfma_f32_16x16x32_bf16 v[104:107], v[196:199], a[56:59], v[104:107]\n" \
  "s_waitcnt vmcnt(16)\n" \
  "v_mul_f32 v82, v82, v82\n" \
  "v_mul_f32 v83, v83, v83\n" \
  "ds_read_b128 a[104:107], v46 offset:26624\n" \
  "s_waitcnt lgkmcnt(11)\n" \
  "v_mfma_f32_16x16x32_bf16 v[108:111], v[196:199], a[60:63], v[108:111]\n" \
  "v_cvt_pk_bf16_f32 v42, v80, v81\n" \
  "v_cvt_pk_bf16_f32 v43, v82, v83\n" \
  "ds_write_b64 v17, v[42:43]\n" \
  "ds_read_b128 a[108:111], v46 offset:27648\n" \
  "s_waitcnt lgkmcnt(12)\n" \
  "v_mfma_f32_16x16x32_bf16 v[96:99], v[200:203], a[64:67], v[96:99]\n" \
  "buffer_load_dwordx4 v[196:199], v8, %[rsW], s40 offen offset:3072 sc1\n" \
  "ds_read_b128 a[112:115], v46 offset:28672\n" \
  "s_waitcnt lgkmcnt(12)\n" \
  "v_mfma_f32_16x16x32_bf16 v[100:103], v[200:203], a[68:71], v[100:103]\n" \
  "v_fma_f32 v38, -v26, v30, v34\n" \
  "v_fma_f32 v39, -v26, v31, v35\n" \
  "ds_read_b128 a[116:119], v46 offset:29696\n" \
  "s_waitcnt lgkmcnt(12)\n" \
  "v_mfma_f32_16x16x32_bf16 v[104:107], v[200:203], a[72:75], v[104:107]\n" \
  "s_waitcnt vmcnt(16)\n" \
  "v_fma_f32 v40, -v26, v32, v36\n" \
  "v_fma_f32 v41, -v26, v33, v37\n" \
  "ds_read_b128 a[120:123], v46 offset:30720\n" \
  "s_waitcnt lgkmcnt(12)\n" \
  "v_mfma_f32_16x16x32_bf16 v[108:111], v[200:203], a[76:79], v[108:111]\n" \
  "v_fma_f32 v84, v22, v84, v38\n" \
  "v_fma_f32 v85, v22, v85, v39\n" \
  "ds_read_b128 a[124:127], v46 offset:31744\n" \
  "s_waitcnt lgkmcnt(12)\n" \
  "v_mfma_f32_16x16x32_bf16 v[96:99], v[204:207], a[80:83], v[96:99]\n" \
  "buffer_load_dwordx4 v[200:203], v9, %[rsW], s40 offen sc1\n" \
  "ds_read_b128 a[128:131], v46 offset:32768\n" \
  "s_waitcnt lgkmcnt(12)\n" \
  "v_mfma_f32_16x16x32_bf16 v[100:103], v[204:207], a[84:87], v[100:103]\n" \
  "v_fma_f32 v86, v22, v86, v40\n" \
  "v_fma_f32 v87, v22, v87, v41\n" \
  "ds_read_b128 a[132:135], v46 offset:33792\n" \
  "s_waitcnt lgkmcnt(12)\n" \
  "v_mfma_f32_16x16x32_bf16 v[104:107], v[204:207], a[88:91], v[104:107]\n" \
  "s_waitcnt vmcnt(16)\n" \
  "v_max_f32 v84, 0, v84\n" \
  "v_max_f32 v85, 0, v85\n" \
  "ds_read_b128 a[136:139], v46 offset:34816\n" \
  "s_waitcnt lgkmcnt(12)\n" \
  "v_mfma_f32_16x16x32_bf16 v[108:111], v[204:207], a[92:95], v[108:111]\n" \
  "v_max_f32 v86, 0, v86\n" \
  "v_max_f32 v87, 0, v87\n" \
  "ds_read_b128 a[140:143], v46 offset:35840\n" \
  "s_waitcnt lgkmcnt(12)\n" \
  "v_mfma_f32_16x16x32_bf16 v[96:99], v[208:211], a[96:99], v[96:99]\n" \
  "buffer_load_dwordx4 v[204:207], v9, %[rsW], s40 offen offset:1024 sc1\n" \
  "ds_read_b128 a[144:147], v46 offset:36864\n" \
  "s_waitcnt lgkmcnt(12)\n" \
  "v_mfma_f32_16x16x32_bf16 v[100:103], v[208:211], a[100:103], v[100:103]\n" \
  "v_mul_f32 v84, v84, v84\n" \
  "v_mul_f32 v85, v85, v85\n" \
  "ds_read_b128 a[148:151], v46 offset:37888\n" \
  "s_waitcnt lgkmcnt(12)\n" \
  "v_mfma_f32_16x16x32_bf16 v[104:107], v[208:211], a[104:107], v[104:107]\n" \
  "s_waitcnt vmcnt(16)\n" \
  "v_mul_f32 v86, v86, v86\n" \
  "v_mul_f32 v87, v87, v87\n" \
  "ds_read_b128 a[152:155], v46 offset:38912\n" \
  "s_waitcnt lgkmcnt(11)\n" \
  "v_mfma_f32_16x16x32_bf16 v[108:111], v[208:211], a[108:111], v[108:111]\n" \
  "v_cvt_pk_bf16_f32 v44, v84, v85\n" \
  "v_cvt_pk_bf16_f32 v45, v86, v87\n" \
  "ds_write_b64 v18, v[44:45]\n" \
  "ds_read_b128 a[156:159], v46 offset:39936\n" \
  "s_waitcnt lgkmcnt(12)\n" \
  "v_mfma_f32_16x16x32_bf16 v[96:99], v[212:215], a[112:115], v[96:99]\n" \
  "buffer_load_dwordx4 v[208:211], v9, %[rsW], s40 offen offset:2048 sc1\n" \
  "ds_read_b128 a[160:163], v46 offset:40960\n" \
  "s_waitcnt lgkmcnt(12)\n" \
  "v_mfma_f32_16x16x32_bf16 v[100:103], v[212:215], a[116:119], v[100:103]\n" \
  "v_fma_f32 v38, -v27, v30, v34\n" \
  "v_fma_f32 v39, -v27, v31, v35\n" \
  "ds_read_b128 a[164:167], v46 offset:41984\n" \
  "s_waitcnt lgkmcnt(12)\n" \
  "v_mfma_f32_16x16x32_bf16 v[104:107], v[212:215], a[120:123], v[104:107]\n" \
  "s_waitcnt vmcnt(16)\n" \
  "v_fma_f32 v40, -v27, v32, v36\n" \
  "v_fma_f32 v41, -v27, v33, v37\n" \
  "ds_read_b128 a[168:171], v46 offset:43008\n" \
  "s_waitcnt lgkmcnt(12)\n" \
  "v_mfma_f32_16x16x32_bf16 v[108:111], v[212:215], a[124:127], v[108:111]\n" \
  "v_fma_f32 v88, v23, v88, v38\n" \
  "v_fma_f32 v89, v23, v89, v39\n" \
  "ds_read_b128 a[172:175], v46 offset:44032\n" \
  "s_waitcnt lgkmcnt(12)\n" \
  "v_mfma_f32_16x16x32_bf16 v[96:99], v[216:219], a[128:131], v[96:99]\n" \
  "buffer_load_dwordx4 v[212:215], v9, %[rsW], s40 offen offset:3072 sc1\n" \
  "ds_read_b128 a[176:179], v46 offset:45056\n" \
  "s_waitcnt lgkmcnt(12)\n" \
  "v_mfma_f32_16x16x32_bf16 v[100:103], v[216:219], a[132:135], v[100:103]\n" \
  "v_fma_f32 v90, v23, v90, v40\n" \
  "v_fma_f32 v91, v23, v91, v41\n" \
  "ds_read_b128 a[180:183], v46 offset:46080\n" \
  "s_waitcnt lgkmcnt(12)\n" \
  "v_mfma_f32_16x16x32_bf16 v[104:107], v[216:219], a[136:139], v[104:107]\n" \
  "s_waitcnt vmcnt(16)\n" \
  "v_max_f32 v88, 0, v88\n" \
  "v_max_f32 v89, 0, v89\n" \
  "ds_read_b128 a[184:187], v46 offset:47104\n" \
  "s_waitcnt lgkmcnt(12)\n" \
  "v_mfma_f32_16x16x32_bf16 v[108:111], v[216:219], a[140:143], v[108:111]\n" \
  "v_max_f32 v90, 0, v90\n" \
  "v_max_f32 v91, 0, v91\n" \
  "ds_read_b128 a[188:191], v46 offset:48128\n" \
  "s_waitcnt lgkmcnt(12)\n" \
  "v_mfma_f32_16x16x32_bf16 v[96:99], v[220:223], a[144:147], v[96:99]\n" \
  "buffer_load_dwordx4 v[216:219], v10, %[rsW], s40 offen sc1\n" \
  "ds_read_b128 a[192:195], v46 offset:49152\n" \
  "s_waitcnt lgkmcnt(12)\n" \
  "v_mfma_f32_16x16x32_bf16 v[100:103], v[220:223], a[148:151], v[100:103]\n" \
  "v_mul_f32 v88, v88, v88\n" \
  "v_mul_f32 v89, v89, v89\n" \
  "ds_read_b128 a[196:199], v46 offset:50176\n" \
  "s_waitcnt lgkmcnt(12)\n" \
  "v_mfma_f32_16x16x32_bf16 v[104:107], v[220:223], a[152:155], v[104:107]\n" \
  "s_waitcnt vmcnt(16)\n" \
  "v_mul_f32 v90, v90, v90\n" \
  "v_mul_f32 v91, v91, v91\n" \
  "ds_read_b128 a[200:203], v46 offset:51200\n" \
  "s_waitcnt lgkmcnt(11)\n" \
  "v_mfma_f32_16x16x32_bf16 v[108:111], v[220:223], a[156:159], v[108:111]\n" \
  "v_cvt_pk_bf16_f32 v42, v88, v89\n" \
  "v_cvt_pk_bf16_f32 v43, v90, v91\n" \
  "ds_write_b64 v19, v[42:43]\n" \
  "ds_read_b128 a[204:207], v46 offset:52224\n" \
  "s_waitcnt lgkmcnt(12)\n" \
  "v_mfma_f32_16x16x32_bf16 v[96:99], v[224:227], a[160:163], v[96:99]\n" \
  "buffer_load_dwordx4 v[220:223], v10, %[rsW], s40 offen offset:1024 sc1\n" \
  "ds_read_b128 a[208:211], v46 offset:53248\n" \
  "s_waitcnt lgkmcnt(12)\n" \
  "v_mfma_f32_16x16x32_bf16 v[100:103], v[224:227], a[164:167], v[100:103]\n" \
  "v_fma_f32 v38, -v28, v30, v34\n" \
  "v_fma_f32 v39, -v28, v31, v35\n" \
  "ds_read_b128 a[212:215], v46 offset:54272\n" \
  "s_waitcnt lgkmcnt(12)\n" \
  "v_mfma_f32_16x16x32_bf16 v[104:107], v[224:227], a[168:171], v[104:107]\n" \
  "s_waitcnt vmcnt(16)\n" \
  "v_fma_f32 v40, -v28, v32, v36\n" \
  "v_fma_f32 v41, -v28, v33, v37\n" \
  "ds_read_b128 a[216:219], v46 offset:55296\n" \
  "s_waitcnt lgkmcnt(12)\n" \
  "v_mfma_f32_16x16x32_bf16 v[108:111], v[224:227], a[172:175], v[108:111]\n" \
  "v_fma_f32 v92, v24, v92, v38\n" \
  "v_fma_f32 v93, v24, v93, v39\n" \
  "ds_read_b128 a[220:223], v46 offset:56320\n" \
  "s_waitcnt lgkmcnt(12)\n" \
  "v_mfma_f32_16x16x32_bf16 v[96:99], v[228:231], a[176:179], v[96:99]\n" \
  "buffer_load_dwordx4 v[224:227], v10, %[rsW], s40 offen offset:2048 sc1\n" \
  "ds_read_b128 a[224:227], v46 offset:57344\n" \
  "s_waitcnt lgkmcnt(12)\n" \
  "v_mfma_f32_16x16x32_bf16 v[100:103], v[228:231], a[180:183], v[100:103]\n" \
  "v_fma_f32 v94, v24, v94, v40\n" \
  "v_fma_f32 v95, v24, v95, v41\n" \
  "ds_read_b128 a[228:231], v46 offset:58368\n" \
  "s_waitcnt lgkmcnt(12)\n" \
  "v_mfma_f32_16x16x32_bf16 v[104:107], v[228:231], a[184:187], v[104:107]\n" \
  "s_waitcnt vmcnt(16)\n" \
  "v_max_f32 v92, 0, v92\n" \
  "v_max_f32 v93, 0, v93\n" \
  "ds_read_b128 a[232:235], v46 offset:59392\n" \
  "s_waitcnt lgkmcnt(12)\n" \
  "v_mfma_f32_16x16x32_bf16 v[108:111], v[228:231], a[188:191], v[108:111]\n" \
  "v_max_f32 v94, 0, v94\n" \
  "v_max_f32 v95, 0, v95\n" \
  "ds_read_b128 a[236:239], v46 offset:60416\n" \
  "s_waitcnt lgkmcnt(12)\n" \
  "v_mfma_f32_16x16x32_bf16 v[96:99], v[232:235], a[192:195], v[96:99]\n" \
  "buffer_load_dwordx4 v[228:231], v10, %[rsW], s40 offen offset:3072 sc1\n" \
  "ds_read_b128 a[240:243], v46 offset:61440\n" \
  "s_waitcnt lgkmcnt(12)\n" \
  "v_mfma_f32_16x16x32_bf16 v[100:103], v[232:235], a[196:199], v[100:103]\n" \
  "v_mul_f32 v92, v92, v92\n" \
  "v_mul_f32 v93, v93, v93\n" \
  "ds_read_b128 a[244:247], v46 offset:62464\n" \
  "s_waitcnt lgkmcnt(12)\n" \
  "v_mfma_f32_16x16x32_bf16 v[104:107], v[232:235], a[200:203], v[104:107]\n" \
  "s_waitcnt vmcnt(16)\n" \
  "v_mul_f32 v94, v94, v94\n" \
  "v_mul_f32 v95, v95, v95\n" \
  "ds_read_b128 a[248:251], v46 offset:63488\n" \
  "s_waitcnt lgkmcnt(11)\n" \
  "v_mfma_f32_16x16x32_bf16 v[108:111], v[232:235], a[204:207], v[108:111]\n" \
  "v_cvt_pk_bf16_f32 v44, v92, v93\n" \
  "v_cvt_pk_bf16_f32 v45, v94, v95\n" \
  "ds_write_b64 v20, v[44:45]\n" \
  "ds_read_b128 a[252:255], v46 offset:64512\n" \
  "s_waitcnt lgkmcnt(12)\n" \
  "v_mfma_f32_16x16x32_bf16 v[96:99], v[236:239], a[208:211], v[96:99]\n" \
  "buffer_load_dwordx4 v[232:235], v11, %[rsW], s40 offen sc1\n" \
  "s_waitcnt lgkmcnt(11)\n" \
  "v_mfma_f32_16x16x32_bf16 v[100:103], v[236:239], a[212:215], v[100:103]\n" \
  "s_waitcnt lgkmcnt(10)\n" \
  "v_mfma_f32_16x16x32_bf16 v[104:107], v[236:239], a[216:219], v[104:107]\n" \
  "s_waitcnt vmcnt(16)\n" \
  "s_waitcnt lgkmcnt(9)\n" \
  "v_mfma_f32_16x16x32_bf16 v[108:111], v[236:239], a[220:223], v[108:111]\n" \
  "s_waitcnt lgkmcnt(1)\n" \
  "s_barrier\n" \
  "ds_read_b128 v[48:51], v47\n" \
  "ds_read_b128 v[52:55], v47 offset:1024\n" \
  "v_mfma_f32_16x16x32_bf16 v[96:99], v[240:243], a[224:227], v[96:99]\n" \
  "buffer_load_dwordx4 v[236:239], v11, %[rsW], s40 offen offset:1024 sc1\n" \
  "ds_read_b128 v[56:59], v47 offset:2048\n" \
  "ds_read_b128 v[60:63], v47 offset:3072\n" \
  "v_mfma_f32_16x16x32_bf16 v[100:103], v[240:243], a[228:231], v[100:103]\n" \
  "ds_read_b128 v[64:67], v47 offset:4096\n" \
  "ds_read_b128 v[68:71], v47 offset:5120\n" \
  "v_mfma_f32_16x16x32_bf16 v[104:107], v[240:243], a[232:235], v[104:107]\n" \
  "s_waitcnt vmcnt(16)\n" \
  "ds_read_b128 v[72:75], v47 offset:6144\n" \
  "ds_read_b128 v[76:79], v47 offset:7168\n" \
  "v_mfma_f32_16x16x32_bf16 v[108:111], v[240:243], a[236:239], v[108:111]\n" \
  "v_mfma_f32_16x16x32_bf16 v[96:99], v[244:247], a[240:243], v[96:99]\n" \
  "buffer_load_dwordx4 v[240:243], v11, %[rsW], s40 offen offset:2048 sc1\n" \
  "v_mfma_f32_16x16x32_bf16 v[100:103], v[244:247], a[244:247], v[100:103]\n" \
  "v_mfma_f32_16x16x32_bf16 v[104:107], v[244:247], a[248:251], v[104:107]\n" \
  "s_waitcnt vmcnt(16)\n" \
  "s_waitcnt lgkmcnt(8)\n" \
  "v_mfma_f32_16x16x32_bf16 v[108:111], v[244:247], a[252:255], v[108:111]\n" \
  "s_waitcnt lgkmcnt(7)\n" \
  "v_mfma_f32_16x16x32_bf16 v[96:99], v[248:251], v[48:51], v[96:99]\n" \
  "buffer_load_dwordx4 v[244:247], v11, %[rsW], s40 offen offset:3072 sc1\n" \
  "s_waitcnt lgkmcnt(6)\n" \
  "v_mfma_f32_16x16x32_bf16 v[100:103], v[248:251], v[52:55], v[100:103]\n" \
  "s_waitcnt lgkmcnt(5)\n" \
  "v_mfma_f32_16x16x32_bf16 v[104:107], v[248:251], v[56:59], v[104:107]\n" \
  "s_waitcnt vmcnt(16)\n" \
  "s_waitcnt lgkmcnt(4)\n" \
  "v_mfma_f32_16x16x32_bf16 v[108:111], v[248:251], v[60:63], v[108:111]\n" \
  "s_waitcnt lgkmcnt(3)\n" \
  "v_mfma_f32_16x16x32_bf16 v[96:99], v[252:255], v[64:67], v[96:99]\n" \
  "buffer_load_dwordx4 v[248:251], v12, %[rsW], s40 offen sc1\n" \
  "s_waitcnt lgkmcnt(2)\n" \
  "v_mfma_f32_16x16x32_bf16 v[100:103], v[252:255], v[68:71], v[100:103]\n" \
  "s_waitcnt lgkmcnt(1)\n" \
  "v_mfma_f32_16x16x32_bf16 v[104:107], v[252:255], v[72:75], v[104:107]\n" \
  "s_waitcnt vmcnt(16)\n" \
  "s_waitcnt lgkmcnt(0)\n" \
  "v_mfma_f32_16x16x32_bf16 v[108:111], v[252:255], v[76:79], v[108:111]\n" \
  "v_mfma_f32_16x16x32_bf16 v[80:83], v[184:187], a[0:3], 0\n" \
  "buffer_load_dwordx4 v[252:255], v12, %[rsW], s40 offen offset:1024 sc1\n" \
  "v_mfma_f32_16x16x32_bf16 v[84:87], v[184:187], a[4:7], 0\n" \
  "v_mfma_f32_16x16x32_bf16 v[88:91], v[184:187], a[8:11], 0\n" \
  "s_waitcnt vmcnt(16)\n" \
  "v_mfma_f32_16x16x32_bf16 v[92:95], v[184:187], a[12:15], 0\n" \
  "v_mfma_f32_16x16x32_bf16 v[80:83], v[188:191], a[16:19], v[80:83]\n" \
  "s_add_u32 s40, s40, 18432\n" \
  "buffer_load_dwordx4 v[184:187], v8, %[rsW], s40 offen sc1\n" \
  "v_mfma_f32_16x16x32_bf16 v[84:87], v[188:191], a[20:23], v[84:87]\n" \
  "v_mfma_f32_16x16x32_bf16 v[88:91], v[188:191], a[24:27], v[88:91]\n" \
  "s_waitcnt vmcnt(16)\n" \
  "v_mfma_f32_16x16x32_bf16 v[92:95], v[188:191], a[28:31], v[92:95]\n" \
  "v_mfma_f32_16x16x32_bf16 v[80:83], v[192:195], a[32:35], v[80:83]\n" \
  "buffer_load_dwordx4 v[188:191], v8, %[rsW], s40 offen offset:1024 sc1\n" \
  "v_mfma_f32_16x16x32_bf16 v[84:87], v[192:195], a[36:39], v[84:87]\n" \
  "v_cvt_pk_bf16_f32 v30, v96, v97\n" \
  "v_mfma_f32_16x16x32_bf16 v[88:91], v[192:195], a[40:43], v[88:91]\n" \
  "s_waitcnt vmcnt(16)\n" \
  "v_mfma_f32_16x16x32_bf16 v[92:95], v[192:195], a[44:47], v[92:95]\n" \
  "v_cvt_pk_bf16_f32 v31, v98, v99\n" \
  "v_mfma_f32_16x16x32_bf16 v[80:83], v[196:199], a[48:51], v[80:83]\n" \
  "buffer_load_dwordx4 v[192:195], v8, %[rsW], s40 offen offset:2048 sc1\n" \
  "v_mfma_f32_16x16x32_bf16 v[84:87], v[196:199], a[52:55], v[84:87]\n" \
  "v_cvt_pk_bf16_f32 v32, v100, v101\n" \
  "v_mfma_f32_16x16x32_bf16 v[88:91], v[196:199], a[56:59], v[88:91]\n" \
  "s_waitcnt vmcnt(16)\n" \
  "v_mfma_f32_16x16x32_bf16 v[92:95], v[196:199], a[60:63], v[92:95]\n" \
  "v_cvt_pk_bf16_f32 v33, v102, v103\n" \
  "v_mfma_f32_16x16x32_bf16 v[80:83], v[200:203], a[64:67], v[80:83]\n" \
  "buffer_load_dwordx4 v[196:199], v8, %[rsW], s40 offen offset:3072 sc1\n" \
  "v_mfma_f32_16x16x32_bf16 v[84:87], v[200:203], a[68:71], v[84:87]\n" \
  "buffer_store_dwordx4 v[30:33], v8, %[rsP], s42 offen sc1\n" \
  "v_mfma_f32_16x16x32_bf16 v[88:91], v[200:203], a[72:75], v[88:91]\n" \
  "s_waitcnt vmcnt(17)\n" \
  "v_mfma_f32_16x16x32_bf16 v[92:95], v[200:203], a[76:79], v[92:95]\n" \
  "v_cvt_pk_bf16_f32 v34, v104, v105\n" \
  "v_mfma_f32_16x16x32_bf16 v[80:83], v[204:207], a[80:83], v[80:83]\n" \
  "buffer_load_dwordx4 v[200:203], v9, %[rsW], s40 offen sc1\n" \
  "v_mfma_f32_16x16x32_bf16 v[84:87], v[204:207], a[84:87], v[84:87]\n" \
  "v_cvt_pk_bf16_f32 v35, v106, v107\n" \
  "v_mfma_f32_16x16x32_bf16 v[88:91], v[204:207], a[88:91], v[88:91]\n" \
  "s_waitcnt vmcnt(17)\n" \
  "v_mfma_f32_16x16x32_bf16 v[92:95], v[204:207], a[92:95], v[92:95]\n" \
  "v_cvt_pk_bf16_f32 v36, v108, v109\n" \
  "v_mfma_f32_16x16x32_bf16 v[80:83], v[208:211], a[96:99], v[80:83]\n" \
  "buffer_load_dwordx4 v[204:207], v9, %[rsW], s40 offen offset:1024 sc1\n" \
  "v_mfma_f32_16x16x32_bf16 v[84:87], v[208:211], a[100:103], v[84:87]\n" \
  "v_cvt_pk_bf16_f32 v37, v110, v111\n" \
  "v_mfma_f32_16x16x32_bf16 v[88:91], v[208:211], a[104:107], v[88:91]\n" \
  "s_waitcnt vmcnt(17)\n" \
  "v_mfma_f32_16x16x32_bf16 v[92:95], v[208:211], a[108:111], v[92:95]\n" \
  "buffer_store_dwordx4 v[34:37], v8, %[rsP], s42 offen offset:1024 sc1\n" \
  "s_add_u32 s42, s42, 2048\n" \
  "v_mfma_f32_16x16x32_bf16 v[80:83], v[212:215], a[112:115], v[80:83]\n" \
  "buffer_load_dwordx4 v[208:211], v9, %[rsW], s40 offen offset:2048 sc1\n" \
  "v_mfma_f32_16x16x32_bf16 v[84:87], v[212:215], a[116:119], v[84:87]\n" \
  "v_mfma_f32_16x16x32_bf16 v[88:91], v[212:215], a[120:123], v[88:91]\n" \
  "s_waitcnt vmcnt(18)\n" \
  "v_mfma_f32_16x16x32_bf16 v[92:95], v[212:215], a[124:127], v[92:95]\n" \
  "v_mfma_f32_16x16x32_bf16 v[80:83], v[216:219], a[128:131], v[80:83]\n" \
  "buffer_load_dwordx4 v[212:215], v9, %[rsW], s40 offen offset:3072 sc1\n" \
  "v_mov_b32 v152, 1\n" \
  "v_mov_b32 v153, 0\n" \
  "s_mov_b32 s53, %[rdA]\n" \
  "v_mfma_f32_16x16x32_bf16 v[84:87], v[216:219], a[132:135], v[84:87]\n" \
  "v_mfma_f32_16x16x32_bf16 v[88:91], v[216:219], a[136:139], v[88:91]\n" \
  "s_waitcnt vmcnt(18)\n" \
  "v_mfma_f32_16x16x32_bf16 v[92:95], v[216:219], a[140:143], v[92:95]\n" \
  "v_mfma_f32_16x16x32_bf16 v[80:83], v[220:223], a[144:147], v[80:83]\n" \
  "buffer_load_dwordx4 v[216:219], v10, %[rsW], s40 offen sc1\n" \
  "v_mfma_f32_16x16x32_bf16 v[84:87], v[220:223], a[148:151], v[84:87]\n" \
  "v_mfma_f32_16x16x32_bf16 v[88:91], v[220:223], a[152:155], v[88:91]\n" \
  "s_waitcnt vmcnt(18)\n" \
  "v_mfma_f32_16x16x32_bf16 v[92:95], v[220:223], a[156:159], v[92:95]\n" \
  "v_mfma_f32_16x16x32_bf16 v[80:83], v[224:227], a[160:163], v[80:83]\n" \
  "buffer_load_dwordx4 v[220:223], v10, %[rsW], s40 offen offset:1024 sc1\n" \
  "v_mfma_f32_16x16x32_bf16 v[84:87], v[224:227], a[164:167], v[84:87]\n" \
  "v_mfma_f32_16x16x32_bf16 v[88:91], v[224:227], a[168:171], v[88:91]\n" \
  "s_waitcnt vmcnt(18)\n" \
  "v_mfma_f32_16x16x32_bf16 v[92:95], v[224:227], a[172:175], v[92:95]\n" \
  "v_mfma_f32_16x16x32_bf16 v[80:83], v[228:231], a[176:179], v[80:83]\n" \
  "buffer_load_dwordx4 v[224:227], v10, %[rsW], s40 offen offset:2048 sc1\n" \
  "v_mfma_f32_16x16x32_bf16 v[84:87], v[228:231], a[180:183], v[84:87]\n" \
  "v_mfma_f32_16x16x32_bf16 v[88:91], v[228:231], a[184:187], v[88:91]\n" \
  "s_waitcnt vmcnt(18)\n" \
  "v_mfma_f32_16x16x32_bf16 v[92:95], v[228:231], a[188:191], v[92:95]\n" \
  "v_mfma_f32_16x16x32_bf16 v[80:83], v[232:235], a[192:195], v[80:83]\n" \
  "buffer_load_dwordx4 v[228:231], v10, %[rsW], s40 offen offset:3072 sc1\n" \
  "v_mfma_f32_16x16x32_bf16 v[84:87], v[232:235], a[196:199], v[84:87]\n" \
  "v_mfma_f32_16x16x32_bf16 v[88:91], v[232:235], a[200:203], v[88:91]\n" \
  "s_waitcnt vmcnt(18)\n" \
  "v_mfma_f32_16x16x32_bf16 v[92:95], v[232:235], a[204:207], v[92:95]\n" \
  "v_mfma_f32_16x16x32_bf16 v[80:83], v[236:239], a[208:211], v[80:83]\n" \
  "buffer_load_dwordx4 v[232:235], v11, %[rsW], s40 offen sc1\n" \
  "v_mfma_f32_16x16x32_bf16 v[84:87], v[236:239], a[212:215], v[84:87]\n" \
  "v_mfma_f32_16x16x32_bf16 v[88:91], v[236:239], a[216:219], v[88:91]\n" \
  "s_waitcnt vmcnt(18)\n" \
  "v_mfma_f32_16x16x32_bf16 v[92:95], v[236:239], a[220:223], v[92:95]\n" \
  "v_mfma_f32_16x16x32_bf16 v[80:83], v[240:243], a[224:227], v[80:83]\n" \
  "buffer_load_dwordx4 v[236:239], v11, %[rsW], s40 offen offset:1024 sc1\n" \
  "v_mfma_f32_16x16x32_bf16 v[84:87], v[240:243], a[228:231], v[84:87]\n" \
  "v_mfma_f32_16x16x32_bf16 v[88:91], v[240:243], a[232:235], v[88:91]\n" \
  "s_waitcnt vmcnt(18)\n" \
  "v_mfma_f32_16x16x32_bf16 v[92:95], v[240:243], a[236:239], v[92:95]\n" \
  "v_mfma_f32_16x16x32_bf16 v[80:83], v[244:247], a[240:243], v[80:83]\n" \
  "buffer_load_dwordx4 v[240:243], v11, %[rsW], s40 offen offset:2048 sc1\n" \
  "v_mfma_f32_16x16x32_bf16 v[84:87], v[244:247], a[244:247], v[84:87]\n" \
  "v_mfma_f32_16x16x32_bf16 v[88:91], v[244:247], a[248:251], v[88:91]\n" \
  "s_waitcnt vmcnt(18)\n" \
  "v_mfma_f32_16x16x32_bf16 v[92:95], v[244:247], a[252:255], v[92:95]\n" \
  "v_mfma_f32_16x16x32_bf16 v[80:83], v[248:251], v[48:51], v[80:83]\n" \
  "buffer_load_dwordx4 v[244:247], v11, %[rsW], s40 offen offset:3072 sc1\n" \
  "v_mfma_f32_16x16x32_bf16 v[84:87], v[248:251], v[52:55], v[84:87]\n" \
  "v_mfma_f32_16x16x32_bf16 v[88:91], v[248:251], v[56:59], v[88:91]\n" \
  "s_waitcnt vmcnt(18)\n" \
  "v_mfma_f32_16x16x32_bf16 v[92:95], v[248:251], v[60:63], v[92:95]\n" \
  "v_mfma_f32_16x16x32_bf16 v[80:83], v[252:255], v[64:67], v[80:83]\n" \
  "buffer_load_dwordx4 v[248:251], v12, %[rsW], s40 offen sc1\n" \
  "v_mfma_f32_16x16x32_bf16 v[84:87], v[252:255], v[68:71], v[84:87]\n" \
  "v_mfma_f32_16x16x32_bf16 v[88:91], v[252:255], v[72:75], v[88:91]\n" \
  "s_waitcnt vmcnt(18)\n" \
  "v_mfma_f32_16x16x32_bf16 v[92:95], v[252:255], v[76:79], v[92:95]\n" \
  "v_mfma_f32_16x16x32_bf16 v[96:99], v[184:187], a[0:3], 0\n" \
  "buffer_load_dwordx4 v[252:255], v12, %[rsW], s40 offen offset:1024 sc1\n" \
  "v_mfma_f32_16x16x32_bf16 v[100:103], v[184:187], a[4:7], 0\n" \
  "v_mfma_f32_16x16x32_bf16 v[104:107], v[184:187], a[8:11], 0\n" \
  "s_waitcnt vmcnt(18)\n" \
  "v_mfma_f32_16x16x32_bf16 v[108:111], v[184:187], a[12:15], 0\n" \
  "v_mfma_f32_16x16x32_bf16 v[96:99], v[188:191], a[16:19], v[96:99]\n" \
  "s_add_u32 s40, s40, 18432\n" \
  "buffer_load_dwordx4 v[184:187], v8, %[rsW], s40 offen sc1\n" \
  "v_mfma_f32_16x16x32_bf16 v[100:103], v[188:191], a[20:23], v[100:103]\n" \
  "v_mfma_f32_16x16x32_bf16 v[104:107], v[188:191], a[24:27], v[104:107]\n" \
  "s_waitcnt vmcnt(18)\n" \
  "v_mfma_f32_16x16x32_bf16 v[108:111], v[188:191], a[28:31], v[108:111]\n" \
  "v_mfma_f32_16x16x32_bf16 v[96:99], v[192:195], a[32:35], v[96:99]\n" \
  "buffer_load_dwordx4 v[188:191], v8, %[rsW], s40 offen offset:1024 sc1\n" \
  "v_mfma_f32_16x16x32_bf16 v[100:103], v[192:195], a[36:39], v[100:103]\n" \
  "v_cvt_pk_bf16_f32 v30, v80, v81\n" \
  "v_mfma_f32_16x16x32_bf16 v[104:107], v[192:195], a[40:43], v[104:107]\n" \
  "s_waitcnt vmcnt(18)\n" \
  "v_mfma_f32_16x16x32_bf16 v[108:111], v[192:195], a[44:47], v[108:111]\n" \
  "v_cvt_pk_bf16_f32 v31, v82, v83\n" \
  "v_mfma_f32_16x16x32_bf16 v[96:99], v[196:199], a[48:51], v[96:99]\n" \
  "buffer_load_dwordx4 v[192:195], v8, %[rsW], s40 offen offset:2048 sc1\n" \
  "v_mfma_f32_16x16x32_bf16 v[100:103], v[196:199], a[52:55], v[100:103]\n" \
  "v_cvt_pk_bf16_f32 v32, v84, v85\n" \
  "v_mfma_f32_16x16x32_bf16 v[104:107], v[196:199], a[56:59], v[104:107]\n" \
  "s_waitcnt vmcnt(17)\n" \
  "v_mfma_f32_16x16x32_bf16 v[108:111], v[196:199], a[60:63], v[108:111]\n" \
  "v_cvt_pk_bf16_f32 v33, v86, v87\n" \
  "v_mfma_f32_16x16x32_bf16 v[96:99], v[200:203], a[64:67], v[96:99]\n" \
  "buffer_load_dwordx4 v[196:199], v8, %[rsW], s40 offen offset:3072 sc1\n" \
  "v_mfma_f32_16x16x32_bf16 v[100:103], v[200:203], a[68:71], v[100:103]\n" \
  "buffer_store_dwordx4 v[30:33], v8, %[rsP], s42 offen sc1\n" \
  "v_mfma_f32_16x16x32_bf16 v[104:107], v[200:203], a[72:75], v[104:107]\n" \
  "s_waitcnt vmcnt(18)\n" \
  "v_mfma_f32_16x16x32_bf16 v[108:111], v[200:203], a[76:79], v[108:111]\n" \
  "v_cvt_pk_bf16_f32 v34, v88, v89\n" \
  "v_mfma_f32_16x16x32_bf16 v[96:99], v[204:207], a[80:83], v[96:99]\n" \
  "buffer_load_dwordx4 v[200:203], v9, %[rsW], s40 offen sc1\n" \
  "v_mfma_f32_16x16x32_bf16 v[100:103], v[204:207], a[84:87], v[100:103]\n" \
  "v_cvt_pk_bf16_f32 v35, v90, v91\n" \
  "v_mfma_f32_16x16x32_bf16 v[104:107], v[204:207], a[88:91], v[104:107]\n" \
  "s_waitcnt vmcnt(17)\n" \
  "v_mfma_f32_16x16x32_bf16 v[108:111], v[204:207], a[92:95], v[108:111]\n" \
  "v_cvt_pk_bf16_f32 v36, v92, v93\n" \
  "v_mfma_f32_16x16x32_bf16 v[96:99], v[208:211], a[96:99], v[96:99]\n" \
  "buffer_load_dwordx4 v[204:207], v9, %[rsW], s40 offen offset:1024 sc1\n" \
  "v_mfma_f32_16x16x32_bf16 v[100:103], v[208:211], a[100:103], v[100:103]\n" \
  "v_cvt_pk_bf16_f32 v37, v94, v95\n" \
  "v_mfma_f32_16x16x32_bf16 v[104:107], v[208:211], a[104:107], v[104:107]\n" \
  "s_waitcnt vmcnt(17)\n" \
  "v_mfma_f32_16x16x32_bf16 v[108:111], v[208:211], a[108:111], v[108:111]\n" \
  "buffer_store_dwordx4 v[34:37], v8, %[rsP], s42 offen offset:1024 sc1\n" \
  "s_add_u32 s42, s42, 2048\n" \
  "v_mfma_f32_16x16x32_bf16 v[96:99], v[212:215], a[112:115], v[96:99]\n" \
  "buffer_load_dwordx4 v[208:211], v9, %[rsW], s40 offen offset:2048 sc1\n" \
  "v_mfma_f32_16x16x32_bf16 v[100:103], v[212:215], a[116:119], v[100:103]\n" \
  "v_mfma_f32_16x16x32_bf16 v[104:107], v[212:215], a[120:123], v[104:107]\n" \
  "s_waitcnt vmcnt(18)\n" \
  "v_mfma_f32_16x16x32_bf16 v[108:111], v[212:215], a[124:127], v[108:111]\n" \
  "v_mfma_f32_16x16x32_bf16 v[96:99], v[216:219], a[128:131], v[96:99]\n" \
  "buffer_load_dwordx4 v[212:215], v9, %[rsW], s40 offen offset:3072 sc1\n" \
  "v_mfma_f32_16x16x32_bf16 v[100:103], v[216:219], a[132:135], v[100:103]\n" \
  "v_mfma_f32_16x16x32_bf16 v[104:107], v[216:219], a[136:139], v[104:107]\n" \
  "s_waitcnt vmcnt(18)\n" \
  "v_mfma_f32_16x16x32_bf16 v[108:111], v[216:219], a[140:143], v[108:111]\n" \
  "v_mfma_f32_16x16x32_bf16 v[96:99], v[220:223], a[144:147], v[96:99]\n" \
  "buffer_load_dwordx4 v[216:219], v10, %[rsW], s40 offen sc1\n" \
  "v_mfma_f32_16x16x32_bf16 v[100:103], v[220:223], a[148:151], v[100:103]\n" \
  "v_mfma_f32_16x16x32_bf16 v[104:107], v[220:223], a[152:155], v[104:107]\n" \
  "s_waitcnt vmcnt(18)\n" \
  "v_mfma_f32_16x16x32_bf16 v[108:111], v[220:223], a[156:159], v[108:111]\n" \
  "v_mfma_f32_16x16x32_bf16 v[96:99], v[224:227], a[160:163], v[96:99]\n" \
  "buffer_load_dwordx4 v[220:223], v10, %[rsW], s40 offen offset:1024 sc1\n" \
  "v_mfma_f32_16x16x32_bf16 v[100:103], v[224:227], a[164:167], v[100:103]\n" \
  "v_mfma_f32_16x16x32_bf16 v[104:107], v[224:227], a[168:171], v[104:107]\n" \
  "s_waitcnt vmcnt(18)\n" \
  "v_mfma_f32_16x16x32_bf16 v[108:111], v[224:227], a[172:175], v[108:111]\n" \
  "v_mfma_f32_16x16x32_bf16 v[96:99], v[228:231], a[176:179], v[96:99]\n" \
  "buffer_load_dwordx4 v[224:227], v10, %[rsW], s40 offen offset:2048 sc1\n" \
  "v_mfma_f32_16x16x32_bf16 v[100:103], v[228:231], a[180:183], v[100:103]\n" \
  "v_mfma_f32_16x16x32_bf16 v[104:107], v[228:231], a[184:187], v[104:107]\n" \
  "s_waitcnt vmcnt(18)\n" \
  "v_mfma_f32_16x16x32_bf16 v[108:111], v[228:231], a[188:191], v[108:111]\n" \
  "v_mfma_f32_16x16x32_bf16 v[96:99], v[232:235], a[192:195], v[96:99]\n" \
  "buffer_load_dwordx4 v[228:231], v10, %[rsW], s40 offen offset:3072 sc1\n" \
  "v_mfma_f32_16x16x32_bf16 v[100:103], v[232:235], a[196:199], v[100:103]\n" \
  "v_mfma_f32_16x16x32_bf16 v[104:107], v[232:235], a[200:203], v[104:107]\n" \
  "s_waitcnt vmcnt(18)\n" \
  "v_mfma_f32_16x16x32_bf16 v[108:111], v[232:235], a[204:207], v[108:111]\n" \
  "v_mfma_f32_16x16x32_bf16 v[96:99], v[236:239], a[208:211], v[96:99]\n" \
  "buffer_load_dwordx4 v[232:235], v11, %[rsW], s40 offen sc1\n" \
  "v_mfma_f32_16x16x32_bf16 v[100:103], v[236:239], a[212:215], v[100:103]\n" \
  "v_mfma_f32_16x16x32_bf16 v[104:107], v[236:239], a[216:219], v[104:107]\n" \
  "s_waitcnt vmcnt(18)\n" \
  "v_mfma_f32_16x16x32_bf16 v[108:111], v[236:239], a[220:223], v[108:111]\n" \
  "v_mfma_f32_16x16x32_bf16 v[96:99], v[240:243], a[224:227], v[96:99]\n" \
  "buffer_load_dwordx4 v[236:239], v11, %[rsW], s40 offen offset:1024 sc1\n" \
  "v_mfma_f32_16x16x32_bf16 v[100:103], v[240:243], a[228:231], v[100:103]\n" \
  "v_mfma_f32_16x16x32_bf16 v[104:107], v[240:243], a[232:235], v[104:107]\n" \
  "s_waitcnt vmcnt(18)\n" \
  "v_mfma_f32_16x16x32_bf16 v[108:111], v[240:243], a[236:239], v[108:111]\n" \
  "v_mfma_f32_16x16x32_bf16 v[96:99], v[244:247], a[240:243], v[96:99]\n" \
  "buffer_load_dwordx4 v[240:243], v11, %[rsW], s40 offen offset:2048 sc1\n" \
  "v_mfma_f32_16x16x32_bf16 v[100:103], v[244:247], a[244:247], v[100:103]\n" \
  "v_mfma_f32_16x16x32_bf16 v[104:107], v[244:247], a[248:251], v[104:107]\n" \
  "s_waitcnt vmcnt(18)\n" \
  "v_mfma_f32_16x16x32_bf16 v[108:111], v[244:247], a[252:255], v[108:111]\n" \
  "v_mfma_f32_16x16x32_bf16 v[96:99], v[248:251], v[48:51], v[96:99]\n" \
  "buffer_load_dwordx4 v[244:247], v11, %[rsW], s40 offen offset:3072 sc1\n" \
  "v_mfma_f32_16x16x32_bf16 v[100:103], v[248:251], v[52:55], v[100:103]\n" \
  "v_mfma_f32_16x16x32_bf16 v[104:107], v[248:251], v[56:59], v[104:107]\n" \
  "s_waitcnt vmcnt(18)\n" \
  "v_mfma_f32_16x16x32_bf16 v[108:111], v[248:251], v[60:63], v[108:111]\n" \
  "v_mfma_f32_16x16x32_bf16 v[96:99], v[252:255], v[64:67], v[96:99]\n" \
  "buffer_load_dwordx4 v[248:251], v12, %[rsW], s40 offen sc1\n" \
  "v_mfma_f32_16x16x32_bf16 v[100:103], v[252:255], v[68:71], v[100:103]\n" \
  "v_mfma_f32_16x16x32_bf16 v[104:107], v[252:255], v[72:75], v[104:107]\n" \
  "s_waitcnt vmcnt(18)\n" \
  "v_mfma_f32_16x16x32_bf16 v[108:111], v[252:255], v[76:79], v[108:111]\n" \
  "v_mfma_f32_16x16x32_bf16 v[80:83], v[184:187], a[0:3], 0\n" \
  "buffer_load_dwordx4 v[252:255], v12, %[rsW], s40 offen offset:1024 sc1\n" \
  "v_mfma_f32_16x16x32_bf16 v[84:87], v[184:187], a[4:7], 0\n" \
  "v_mfma_f32_16x16x32_bf16 v[88:91], v[184:187], a[8:11], 0\n" \
  "s_waitcnt vmcnt(18)\n" \
  "v_mfma_f32_16x16x32_bf16 v[92:95], v[184:187], a[12:15], 0\n" \
  "v_mfma_f32_16x16x32_bf16 v[80:83], v[188:191], a[16:19], v[80:83]\n" \
  "s_add_u32 s40, s40, 18432\n" \
  "buffer_load_dwordx4 v[184:187], v8, %[rsW], s40 offen sc1\n" \
  "v_mfma_f32_16x16x32_bf16 v[84:87], v[188:191], a[20:23], v[84:87]\n" \
  "v_mfma_f32_16x16x32_bf16 v[88:91], v[188:191], a[24:27], v[88:91]\n" \
  "s_waitcnt vmcnt(18)\n" \
  "v_mfma_f32_16x16x32_bf16 v[92:95], v[188:191], a[28:31], v[92:95]\n" \
  "v_mfma_f32_16x16x32_bf16 v[80:83], v[192:195], a[32:35], v[80:83]\n" \
  "buffer_load_dwordx4 v[188:191], v8, %[rsW], s40 offen offset:1024 sc1\n" \
  "v_mfma_f32_16x16x32_bf16 v[84:87], v[192:195], a[36:39], v[84:87]\n" \
  "v_cvt_pk_bf16_f32 v30, v96, v97\n" \
  "v_mfma_f32_16x16x32_bf16 v[88:91], v[192:195], a[40:43], v[88:91]\n" \
  "s_waitcnt vmcnt(18)\n" \
  "v_mfma_f32_16x16x32_bf16 v[92:95], v[192:195], a[44:47], v[92:95]\n" \
  "v_cvt_pk_bf16_f32 v31, v98, v99\n" \
  "v_mfma_f32_16x16x32_bf16 v[80:83], v[196:199], a[48:51], v[80:83]\n" \
  "buffer_load_dwordx4 v[192:195], v8, %[rsW], s40 offen offset:2048 sc1\n" \
  "v_mfma_f32_16x16x32_bf16 v[84:87], v[196:199], a[52:55], v[84:87]\n" \
  "v_cvt_pk_bf16_f32 v32, v100, v101\n" \
  "v_mfma_f32_16x16x32_bf16 v[88:91], v[196:199], a[56:59], v[88:91]\n" \
  "s_waitcnt vmcnt(17)\n" \
  "v_mfma_f32_16x16x32_bf16 v[92:95], v[196:199], a[60:63], v[92:95]\n" \
  "v_cvt_pk_bf16_f32 v33, v102, v103\n" \
  "v_mfma_f32_16x16x32_bf16 v[80:83], v[200:203], a[64:67], v[80:83]\n" \
  "buffer_load_dwordx4 v[196:199], v8, %[rsW], s40 offen offset:3072 sc1\n" \
  "v_mfma_f32_16x16x32_bf16 v[84:87], v[200:203], a[68:71], v[84:87]\n" \
  "buffer_store_dwordx4 v[30:33], v8, %[rsP], s42 offen sc1\n" \
  "v_mfma_f32_16x16x32_bf16 v[88:91], v[200:203], a[72:75], v[88:91]\n" \
  "s_waitcnt vmcnt(18)\n" \
  "v_mfma_f32_16x16x32_bf16 v[92:95], v[200:203], a[76:79], v[92:95]\n" \
  "v_cvt_pk_bf16_f32 v34, v104, v105\n" \
  "v_mfma_f32_16x16x32_bf16 v[80:83], v[204:207], a[80:83], v[80:83]\n" \
  "buffer_load_dwordx4 v[200:203], v9, %[rsW], s40 offen sc1\n" \
  "v_mfma_f32_16x16x32_bf16 v[84:87], v[204:207], a[84:87], v[84:87]\n" \
  "v_cvt_pk_bf16_f32 v35, v106, v107\n" \
  "v_mfma_f32_16x16x32_bf16 v[88:91], v[204:207], a[88:91], v[88:91]\n" \
  "s_waitcnt vmcnt(17)\n" \
  "v_mfma_f32_16x16x32_bf16 v[92:95], v[204:207], a[92:95], v[92:95]\n" \
  "v_cvt_pk_bf16_f32 v36, v108, v109\n" \
  "v_mfma_f32_16x16x32_bf16 v[80:83], v[208:211], a[96:99], v[80:83]\n" \
  "buffer_load_dwordx4 v[204:207], v9, %[rsW], s40 offen offset:1024 sc1\n" \
  "v_mfma_f32_16x16x32_bf16 v[84:87], v[208:211], a[100:103], v[84:87]\n" \
  "v_cvt_pk_bf16_f32 v37, v110, v111\n" \
  "v_mfma_f32_16x16x32_bf16 v[88:91], v[208:211], a[104:107], v[88:91]\n" \
  "s_waitcnt vmcnt(17)\n" \
  "v_mfma_f32_16x16x32_bf16 v[92:95], v[208:211], a[108:111], v[92:95]\n" \
  "buffer_store_dwordx4 v[34:37], v8, %[rsP], s42 offen offset:1024 sc1\n" \
  "s_add_u32 s42, s42, 2048\n" \
  "v_mfma_f32_16x16x32_bf16 v[80:83], v[212:215], a[112:115], v[80:83]\n" \
  "buffer_load_dwordx4 v[208:211], v9, %[rsW], s40 offen offset:2048 sc1\n" \
  "v_mfma_f32_16x16x32_bf16 v[84:87], v[212:215], a[116:119], v[84:87]\n" \
  "v_mfma_f32_16x16x32_bf16 v[88:91], v[212:215], a[120:123], v[88:91]\n" \
  "s_waitcnt vmcnt(18)\n" \
  "v_mfma_f32_16x16x32_bf16 v[92:95], v[212:215], a[124:127], v[92:95]\n" \
  "v_mfma_f32_16x16x32_bf16 v[80:83], v[216:219], a[128:131], v[80:83]\n" \
  "buffer_load_dwordx4 v[212:215], v9, %[rsW], s40 offen offset:3072 sc1\n" \
  "v_mfma_f32_16x16x32_bf16 v[84:87], v[216:219], a[132:135], v[84:87]\n" \
  "v_mfma_f32_16x16x32_bf16 v[88:91], v[216:219], a[136:139], v[88:91]\n" \
  "s_waitcnt vmcnt(18)\n" \
  "v_mfma_f32_16x16x32_bf16 v[92:95], v[216:219], a[140:143], v[92:95]\n" \
  "v_mfma_f32_16x16x32_bf16 v[80:83], v[220:223], a[144:147], v[80:83]\n" \
  "buffer_load_dwordx4 v[216:219], v10, %[rsW], s40 offen sc1\n" \
  "v_mfma_f32_16x16x32_bf16 v[84:87], v[220:223], a[148:151], v[84:87]\n" \
  "v_mfma_f32_16x16x32_bf16 v[88:91], v[220:223], a[152:155], v[88:91]\n" \
  "s_waitcnt vmcnt(18)\n" \
  "v_mfma_f32_16x16x32_bf16 v[92:95], v[220:223], a[156:159], v[92:95]\n" \
  "v_mfma_f32_16x16x32_bf16 v[80:83], v[224:227], a[160:163], v[80:83]\n" \
  "buffer_load_dwordx4 v[220:223], v10, %[rsW], s40 offen offset:1024 sc1\n" \
  "v_mfma_f32_16x16x32_bf16 v[84:87], v[224:227], a[164:167], v[84:87]\n" \
  "v_mfma_f32_16x16x32_bf16 v[88:91], v[224:227], a[168:171], v[88:91]\n" \
  "s_waitcnt vmcnt(18)\n" \
  "v_mfma_f32_16x16x32_bf16 v[92:95], v[224:227], a[172:175], v[92:95]\n" \
  "v_mfma_f32_16x16x32_bf16 v[80:83], v[228:231], a[176:179], v[80:83]\n" \
  "buffer_load_dwordx4 v[224:227], v10, %[rsW], s40 offen offset:2048 sc1\n" \
  "v_mfma_f32_16x16x32_bf16 v[84:87], v[228:231], a[180:183], v[84:87]\n" \
  "v_mfma_f32_16x16x32_bf16 v[88:91], v[228:231], a[184:187], v[88:91]\n" \
  "s_waitcnt vmcnt(18)\n" \
  "v_mfma_f32_16x16x32_bf16 v[92:95], v[228:231], a[188:191], v[92:95]\n" \
  "v_mfma_f32_16x16x32_bf16 v[80:83], v[232:235], a[192:195], v[80:83]\n" \
  "buffer_load_dwordx4 v[228:231], v10, %[rsW], s40 offen offset:3072 sc1\n" \
  "v_mfma_f32_16x16x32_bf16 v[84:87], v[232:235], a[196:199], v[84:87]\n" \
  "v_mfma_f32_16x16x32_bf16 v[88:91], v[232:235], a[200:203], v[88:91]\n" \
  "s_waitcnt vmcnt(18)\n" \
  "v_mfma_f32_16x16x32_bf16 v[92:95], v[232:235], a[204:207], v[92:95]\n" \
  "v_mfma_f32_16x16x32_bf16 v[80:83], v[236:239], a[208:211], v[80:83]\n" \
  "buffer_load_dwordx4 v[232:235], v11, %[rsW], s40 offen sc1\n" \
  "v_mfma_f32_16x16x32_bf16 v[84:87], v[236:239], a[212:215], v[84:87]\n" \
  "v_mfma_f32_16x16x32_bf16 v[88:91], v[236:239], a[216:219], v[88:91]\n" \
  "s_waitcnt vmcnt(18)\n" \
  "v_mfma_f32_16x16x32_bf16 v[92:95], v[236:239], a[220:223], v[92:95]\n" \
  "v_mfma_f32_16x16x32_bf16 v[80:83], v[240:243], a[224:227], v[80:83]\n" \
  "buffer_load_dwordx4 v[236:239], v11, %[rsW], s40 offen offset:1024 sc1\n" \
  "v_mfma_f32_16x16x32_bf16 v[84:87], v[240:243], a[228:231], v[84:87]\n" \
  "v_mfma_f32_16x16x32_bf16 v[88:91], v[240:243], a[232:235], v[88:91]\n" \
  "s_waitcnt vmcnt(18)\n" \
  "v_mfma_f32_16x16x32_bf16 v[92:95], v[240:243], a[236:239], v[92:95]\n" \
  "v_mfma_f32_16x16x32_bf16 v[80:83], v[244:247], a[240:243], v[80:83]\n" \
  "buffer_load_dwordx4 v[240:243], v11, %[rsW], s40 offen offset:2048 sc1\n" \
  "v_mfma_f32_16x16x32_bf16 v[84:87], v[244:247], a[244:247], v[84:87]\n" \
  "v_mfma_f32_16x16x32_bf16 v[88:91], v[244:247], a[248:251], v[88:91]\n" \
  "s_waitcnt vmcnt(18)\n" \
  "v_mfma_f32_16x16x32_bf16 v[92:95], v[244:247], a[252:255], v[92:95]\n" \
  "v_mfma_f32_16x16x32_bf16 v[80:83], v[248:251], v[48:51], v[80:83]\n" \
  "buffer_load_dwordx4 v[244:247], v11, %[rsW], s40 offen offset:3072 sc1\n" \
  "v_mfma_f32_16x16x32_bf16 v[84:87], v[248:251], v[52:55], v[84:87]\n" \
  "v_mfma_f32_16x16x32_bf16 v[88:91], v[248:251], v[56:59], v[88:91]\n" \
  "s_waitcnt vmcnt(18)\n" \
  "v_mfma_f32_16x16x32_bf16 v[92:95], v[248:251], v[60:63], v[92:95]\n" \
  "v_mfma_f32_16x16x32_bf16 v[80:83], v[252:255], v[64:67], v[80:83]\n" \
  "buffer_load_dwordx4 v[248:251], v12, %[rsW], s40 offen sc1\n" \
  "v_mfma_f32_16x16x32_bf16 v[84:87], v[252:255], v[68:71], v[84:87]\n" \
  "v_mfma_f32_16x16x32_bf16 v[88:91], v[252:255], v[72:75], v[88:91]\n" \
  "s_waitcnt vmcnt(18)\n" \
  "v_mfma_f32_16x16x32_bf16 v[92:95], v[252:255], v[76:79], v[92:95]\n" \
  "v_mfma_f32_16x16x32_bf16 v[96:99], v[184:187], a[0:3], 0\n" \
  "buffer_load_dwordx4 v[252:255], v12, %[rsW], s40 offen offset:1024 sc1\n" \
  "v_mfma_f32_16x16x32_bf16 v[100:103], v[184:187], a[4:7], 0\n" \
  "v_mfma_f32_16x16x32_bf16 v[104:107], v[184:187], a[8:11], 0\n" \
  "s_waitcnt vmcnt(18)\n" \
  "v_mfma_f32_16x16x32_bf16 v[108:111], v[184:187], a[12:15], 0\n" \
  "v_mfma_f32_16x16x32_bf16 v[96:99], v[188:191], a[16:19], v[96:99]\n" \
  "s_add_u32 s40, s40, 18432\n" \
  "buffer_load_dwordx4 v[184:187], v8, %[rsW], s40 offen sc1\n" \
  "v_mfma_f32_16x16x32_bf16 v[100:103], v[188:191], a[20:23], v[100:103]\n" \
  "v_mfma_f32_16x16x32_bf16 v[104:107], v[188:191], a[24:27], v[104:107]\n" \
  "s_waitcnt vmcnt(18)\n" \
  "v_mfma_f32_16x16x32_bf16 v[108:111], v[188:191], a[28:31], v[108:111]\n" \
  "v_mfma_f32_16x16x32_bf16 v[96:99], v[192:195], a[32:35], v[96:99]\n" \
  "buffer_load_dwordx4 v[188:191], v8, %[rsW], s40 offen offset:1024 sc1\n" \
  "v_mfma_f32_16x16x32_bf16 v[100:103], v[192:195], a[36:39], v[100:103]\n" \
  "v_cvt_pk_bf16_f32 v30, v80, v81\n" \
  "v_mfma_f32_16x16x32_bf16 v[104:107], v[192:195], a[40:43], v[104:107]\n" \
  "s_waitcnt vmcnt(18)\n" \
  "v_mfma_f32_16x16x32_bf16 v[108:111], v[192:195], a[44:47], v[108:111]\n" \
  "v_cvt_pk_bf16_f32 v31, v82, v83\n" \
  "v_mfma_f32_16x16x32_bf16 v[96:99], v[196:199], a[48:51], v[96:99]\n" \
  "buffer_load_dwordx4 v[192:195], v8, %[rsW], s40 offen offset:2048 sc1\n" \
  "v_mfma_f32_16x16x32_bf16 v[100:103], v[196:199], a[52:55], v[100:103]\n" \
  "v_cvt_pk_bf16_f32 v32, v84, v85\n" \
  "v_mfma_f32_16x16x32_bf16 v[104:107], v[196:199], a[56:59], v[104:107]\n" \
  "s_waitcnt vmcnt(17)\n" \
  "v_mfma_f32_16x16x32_bf16 v[108:111], v[196:199], a[60:63], v[108:111]\n" \
  "v_cvt_pk_bf16_f32 v33, v86, v87\n" \
  "v_mfma_f32_16x16x32_bf16 v[96:99], v[200:203], a[64:67], v[96:99]\n" \
  "buffer_load_dwordx4 v[196:199], v8, %[rsW], s40 offen offset:3072 sc1\n" \
  "v_mfma_f32_16x16x32_bf16 v[100:103], v[200:203], a[68:71], v[100:103]\n" \
  "buffer_store_dwordx4 v[30:33], v8, %[rsP], s42 offen sc1\n" \
  "v_mfma_f32_16x16x32_bf16 v[104:107], v[200:203], a[72:75], v[104:107]\n" \
  "s_waitcnt vmcnt(18)\n" \
  "v_mfma_f32_16x16x32_bf16 v[108:111], v[200:203], a[76:79], v[108:111]\n" \
  "v_cvt_pk_bf16_f32 v34, v88, v89\n" \
  "v_mfma_f32_16x16x32_bf16 v[96:99], v[204:207], a[80:83], v[96:99]\n" \
  "buffer_load_dwordx4 v[200:203], v9, %[rsW], s40 offen sc1\n" \
  "v_mfma_f32_16x16x32_bf16 v[100:103], v[204:207], a[84:87], v[100:103]\n" \
  "v_cvt_pk_bf16_f32 v35, v90, v91\n" \
  "v_mfma_f32_16x16x32_bf16 v[104:107], v[204:207], a[88:91], v[104:107]\n" \
  "s_waitcnt vmcnt(17)\n" \
  "v_mfma_f32_16x16x32_bf16 v[108:111], v[204:207], a[92:95], v[108:111]\n" \
  "v_cvt_pk_bf16_f32 v36, v92, v93\n" \
  "v_mfma_f32_16x16x32_bf16 v[96:99], v[208:211], a[96:99], v[96:99]\n" \
  "buffer_load_dwordx4 v[204:207], v9, %[rsW], s40 offen offset:1024 sc1\n" \
  "v_mfma_f32_16x16x32_bf16 v[100:103], v[208:211], a[100:103], v[100:103]\n" \
  "v_cvt_pk_bf16_f32 v37, v94, v95\n" \
  "v_mfma_f32_16x16x32_bf16 v[104:107], v[208:211], a[104:107], v[104:107]\n" \
  "s_waitcnt vmcnt(17)\n" \
  "v_mfma_f32_16x16x32_bf16 v[108:111], v[208:211], a[108:111], v[108:111]\n" \
  "buffer_store_dwordx4 v[34:37], v8, %[rsP], s42 offen offset:1024 sc1\n" \
  "s_add_u32 s42, s42, 2048\n" \
  "v_mfma_f32_16x16x32_bf16 v[96:99], v[212:215], a[112:115], v[96:99]\n" \
  "buffer_load_dwordx4 v[208:211], v9, %[rsW], s40 offen offset:2048 sc1\n" \
  "v_mfma_f32_16x16x32_bf16 v[100:103], v[212:215], a[116:119], v[100:103]\n" \
  "v_mfma_f32_16x16x32_bf16 v[104:107], v[212:215], a[120:123], v[104:107]\n" \
  "s_waitcnt vmcnt(18)\n" \
  "v_mfma_f32_16x16x32_bf16 v[108:111], v[212:215], a[124:127], v[108:111]\n" \
  "v_mfma_f32_16x16x32_bf16 v[96:99], v[216:219], a[128:131], v[96:99]\n" \
  "buffer_load_dwordx4 v[212:215], v9, %[rsW], s40 offen offset:3072 sc1\n" \
  "v_mfma_f32_16x16x32_bf16 v[100:103], v[216:219], a[132:135], v[100:103]\n" \
  "v_mfma_f32_16x16x32_bf16 v[104:107], v[216:219], a[136:139], v[104:107]\n" \
  "s_waitcnt vmcnt(18)\n" \
  "v_mfma_f32_16x16x32_bf16 v[108:111], v[216:219], a[140:143], v[108:111]\n" \
  "v_mfma_f32_16x16x32_bf16 v[96:99], v[220:223], a[144:147], v[96:99]\n" \
  "buffer_load_dwordx4 v[216:219], v10, %[rsW], s40 offen sc1\n" \
  "v_mfma_f32_16x16x32_bf16 v[100:103], v[220:223], a[148:151], v[100:103]\n" \
  "v_mfma_f32_16x16x32_bf16 v[104:107], v[220:223], a[152:155], v[104:107]\n" \
  "s_waitcnt vmcnt(18)\n" \
  "v_mfma_f32_16x16x32_bf16 v[108:111], v[220:223], a[156:159], v[108:111]\n" \
  "v_mfma_f32_16x16x32_bf16 v[96:99], v[224:227], a[160:163], v[96:99]\n" \
  "buffer_load_dwordx4 v[220:223], v10, %[rsW], s40 offen offset:1024 sc1\n" \
  "v_mfma_f32_16x16x32_bf16 v[100:103], v[224:227], a[164:167], v[100:103]\n" \
  "v_mfma_f32_16x16x32_bf16 v[104:107], v[224:227], a[168:171], v[104:107]\n" \
  "s_waitcnt vmcnt(18)\n" \
  "v_mfma_f32_16x16x32_bf16 v[108:111], v[224:227], a[172:175], v[108:111]\n" \
  "v_mfma_f32_16x16x32_bf16 v[96:99], v[228:231], a[176:179], v[96:99]\n" \
  "buffer_load_dwordx4 v[224:227], v10, %[rsW], s40 offen offset:2048 sc1\n" \
  "v_mfma_f32_16x16x32_bf16 v[100:103], v[228:231], a[180:183], v[100:103]\n" \
  "v_mfma_f32_16x16x32_bf16 v[104:107], v[228:231], a[184:187], v[104:107]\n" \
  "s_waitcnt vmcnt(18)\n" \
  "v_mfma_f32_16x16x32_bf16 v[108:111], v[228:231], a[188:191], v[108:111]\n" \
  "v_mfma_f32_16x16x32_bf16 v[96:99], v[232:235], a[192:195], v[96:99]\n" \
  "buffer_load_dwordx4 v[228:231], v10, %[rsW], s40 offen offset:3072 sc1\n" \
  "v_mfma_f32_16x16x32_bf16 v[100:103], v[232:235], a[196:199], v[100:103]\n" \
  "v_mfma_f32_16x16x32_bf16 v[104:107], v[232:235], a[200:203], v[104:107]\n" \
  "s_waitcnt vmcnt(18)\n" \
  "v_mfma_f32_16x16x32_bf16 v[108:111], v[232:235], a[204:207], v[108:111]\n" \
  "v_mfma_f32_16x16x32_bf16 v[96:99], v[236:239], a[208:211], v[96:99]\n" \
  "buffer_load_dwordx4 v[232:235], v11, %[rsW], s40 offen sc1\n" \
  "v_mfma_f32_16x16x32_bf16 v[100:103], v[236:239], a[212:215], v[100:103]\n" \
  "v_mfma_f32_16x16x32_bf16 v[104:107], v[236:239], a[216:219], v[104:107]\n" \
  "s_waitcnt vmcnt(18)\n" \
  "v_mfma_f32_16x16x32_bf16 v[108:111], v[236:239], a[220:223], v[108:111]\n" \
  "v_mfma_f32_16x16x32_bf16 v[96:99], v[240:243], a[224:227], v[96:99]\n" \
  "buffer_load_dwordx4 v[236:239], v11, %[rsW], s40 offen offset:1024 sc1\n" \
  "v_mfma_f32_16x16x32_bf16 v[100:103], v[240:243], a[228:231], v[100:103]\n" \
  "v_mfma_f32_16x16x32_bf16 v[104:107], v[240:243], a[232:235], v[104:107]\n" \
  "s_waitcnt vmcnt(18)\n" \
  "v_mfma_f32_16x16x32_bf16 v[108:111], v[240:243], a[236:239], v[108:111]\n" \
  "v_mfma_f32_16x16x32_bf16 v[96:99], v[244:247], a[240:243], v[96:99]\n" \
  "buffer_load_dwordx4 v[240:243], v11, %[rsW], s40 offen offset:2048 sc1\n" \
  "v_mfma_f32_16x16x32_bf16 v[100:103], v[244:247], a[244:247], v[100:103]\n" \
  "v_mfma_f32_16x16x32_bf16 v[104:107], v[244:247], a[248:251], v[104:107]\n" \
  "s_waitcnt vmcnt(18)\n" \
  "v_mfma_f32_16x16x32_bf16 v[108:111], v[244:247], a[252:255], v[108:111]\n" \
  "v_mfma_f32_16x16x32_bf16 v[96:99], v[248:251], v[48:51], v[96:99]\n" \
  "buffer_load_dwordx4 v[244:247], v11, %[rsW], s40 offen offset:3072 sc1\n" \
  "v_mfma_f32_16x16x32_bf16 v[100:103], v[248:251], v[52:55], v[100:103]\n" \
  "v_mfma_f32_16x16x32_bf16 v[104:107], v[248:251], v[56:59], v[104:107]\n" \
  "s_waitcnt vmcnt(18)\n" \
  "v_mfma_f32_16x16x32_bf16 v[108:111], v[248:251], v[60:63], v[108:111]\n" \
  "v_mfma_f32_16x16x32_bf16 v[96:99], v[252:255], v[64:67], v[96:99]\n" \
  "buffer_load_dwordx4 v[248:251], v12, %[rsW], s40 offen sc1\n" \
  "v_mfma_f32_16x16x32_bf16 v[100:103], v[252:255], v[68:71], v[100:103]\n" \
  "v_mfma_f32_16x16x32_bf16 v[104:107], v[252:255], v[72:75], v[104:107]\n" \
  "s_waitcnt vmcnt(18)\n" \
  "v_mfma_f32_16x16x32_bf16 v[108:111], v[252:255], v[76:79], v[108:111]\n" \
  "v_mfma_f32_16x16x32_bf16 v[80:83], v[184:187], a[0:3], 0\n" \
  "buffer_load_dwordx4 v[252:255], v12, %[rsW], s40 offen offset:1024 sc1\n" \
  "v_mfma_f32_16x16x32_bf16 v[84:87], v[184:187], a[4:7], 0\n" \
  "v_mfma_f32_16x16x32_bf16 v[88:91], v[184:187], a[8:11], 0\n" \
  "s_waitcnt vmcnt(18)\n" \
  "v_mfma_f32_16x16x32_bf16 v[92:95], v[184:187], a[12:15], 0\n" \
  "v_mfma_f32_16x16x32_bf16 v[80:83], v[188:191], a[16:19], v[80:83]\n" \
  "s_add_u32 s40, s40, 18432\n" \
  "buffer_load_dwordx4 v[184:187], v8, %[rsW], s40 offen sc1\n" \
  "v_mfma_f32_16x16x32_bf16 v[84:87], v[188:191], a[20:23], v[84:87]\n" \
  "v_mfma_f32_16x16x32_bf16 v[88:91], v[188:191], a[24:27], v[88:91]\n" \
  "s_waitcnt vmcnt(18)\n" \
  "v_mfma_f32_16x16x32_bf16 v[92:95], v[188:191], a[28:31], v[92:95]\n" \
  "v_mfma_f32_16x16x32_bf16 v[80:83], v[192:195], a[32:35], v[80:83]\n" \
  "buffer_load_dwordx4 v[188:191], v8, %[rsW], s40 offen offset:1024 sc1\n" \
  "v_mfma_f32_16x16x32_bf16 v[84:87], v[192:195], a[36:39], v[84:87]\n" \
  "v_cvt_pk_bf16_f32 v30, v96, v97\n" \
  "v_mfma_f32_16x16x32_bf16 v[88:91], v[192:195], a[40:43], v[88:91]\n" \
  "s_waitcnt vmcnt(18)\n" \
  "v_mfma_f32_16x16x32_bf16 v[92:95], v[192:195], a[44:47], v[92:95]\n" \
  "v_cvt_pk_bf16_f32 v31, v98, v99\n" \
  "v_mfma_f32_16x16x32_bf16 v[80:83], v[196:199], a[48:51], v[80:83]\n" \
  "buffer_load_dwordx4 v[192:195], v8, %[rsW], s40 offen offset:2048 sc1\n" \
  "v_mfma_f32_16x16x32_bf16 v[84:87], v[196:199], a[52:55], v[84:87]\n" \
  "v_cvt_pk_bf16_f32 v32, v100, v101\n" \
  "v_mfma_f32_16x16x32_bf16 v[88:91], v[196:199], a[56:59], v[88:91]\n" \
  "s_waitcnt vmcnt(17)\n" \
  "v_mfma_f32_16x16x32_bf16 v[92:95], v[196:199], a[60:63], v[92:95]\n" \
  "v_cvt_pk_bf16_f32 v33, v102, v103\n" \
  "v_mfma_f32_16x16x32_bf16 v[80:83], v[200:203], a[64:67], v[80:83]\n" \
  "buffer_load_dwordx4 v[196:199], v8, %[rsW], s40 offen offset:3072 sc1\n" \
  "v_mfma_f32_16x16x32_bf16 v[84:87], v[200:203], a[68:71], v[84:87]\n" \
  "buffer_store_dwordx4 v[30:33], v8, %[rsP], s42 offen sc1\n" \
  "v_mfma_f32_16x16x32_bf16 v[88:91], v[200:203], a[72:75], v[88:91]\n" \
  "s_waitcnt vmcnt(18)\n" \
  "v_mfma_f32_16x16x32_bf16 v[92:95], v[200:203], a[76:79], v[92:95]\n" \
  "v_cvt_pk_bf16_f32 v34, v104, v105\n" \
  "v_mfma_f32_16x16x32_bf16 v[80:83], v[204:207], a[80:83], v[80:83]\n" \
  "buffer_load_dwordx4 v[200:203], v9, %[rsW], s40 offen sc1\n" \
  "v_mfma_f32_16x16x32_bf16 v[84:87], v[204:207], a[84:87], v[84:87]\n" \
  "v_cvt_pk_bf16_f32 v35, v106, v107\n" \
  "v_mfma_f32_16x16x32_bf16 v[88:91], v[204:207], a[88:91], v[88:91]\n" \
  "s_waitcnt vmcnt(17)\n" \
  "v_mfma_f32_16x16x32_bf16 v[92:95], v[204:207], a[92:95], v[92:95]\n" \
  "v_cvt_pk_bf16_f32 v36, v108, v109\n" \
  "v_mfma_f32_16x16x32_bf16 v[80:83], v[208:211], a[96:99], v[80:83]\n" \
  "buffer_load_dwordx4 v[204:207], v9, %[rsW], s40 offen offset:1024 sc1\n" \
  "v_mfma_f32_16x16x32_bf16 v[84:87], v[208:211], a[100:103], v[84:87]\n" \
  "v_cvt_pk_bf16_f32 v37, v110, v111\n" \
  "v_mfma_f32_16x16x32_bf16 v[88:91], v[208:211], a[104:107], v[88:91]\n" \
  "s_waitcnt vmcnt(17)\n" \
  "v_mfma_f32_16x16x32_bf16 v[92:95], v[208:211], a[108:111], v[92:95]\n" \
  "buffer_store_dwordx4 v[34:37], v8, %[rsP], s42 offen offset:1024 sc1\n" \
  "s_add_u32 s42, s42, 2048\n" \
  "v_mfma_f32_16x16x32_bf16 v[80:83], v[212:215], a[112:115], v[80:83]\n" \
  "buffer_load_dwordx4 v[208:211], v9, %[rsW], s40 offen offset:2048 sc1\n" \
  "v_mfma_f32_16x16x32_bf16 v[84:87], v[212:215], a[116:119], v[84:87]\n" \
  "s_mov_b64 exec, 1\n" \
  "buffer_atomic_add v152, v153, %[rsF], 0 offen\n" \
  "s_mov_b64 exec, -1\n" \
  "s_nop 3\n" \
  "v_mfma_f32_16x16x32_bf16 v[88:91], v[212:215], a[120:123], v[88:91]\n" \
  "s_waitcnt vmcnt(19)\n" \
  "v_mfma_f32_16x16x32_bf16 v[92:95], v[212:215], a[124:127], v[92:95]\n" \
  "v_mfma_f32_16x16x32_bf16 v[80:83], v[216:219], a[128:131], v[80:83]\n" \
  "buffer_load_dwordx4 v[212:215], v9, %[rsW], s40 offen offset:3072 sc1\n" \
  "v_mfma_f32_16x16x32_bf16 v[84:87], v[216:219], a[132:135], v[84:87]\n" \
  "v_mfma_f32_16x16x32_bf16 v[88:91], v[216:219], a[136:139], v[88:91]\n" \
  "s_waitcnt vmcnt(19)\n" \
  "v_mfma_f32_16x16x32_bf16 v[92:95], v[216:219], a[140:143], v[92:95]\n" \
  "v_mfma_f32_16x16x32_bf16 v[80:83], v[220:223], a[144:147], v[80:83]\n" \
  "buffer_load_dwordx4 v[216:219], v10, %[rsW], s40 offen sc1\n" \
  "v_mfma_f32_16x16x32_bf16 v[84:87], v[220:223], a[148:151], v[84:87]\n" \
  "v_mfma_f32_16x16x32_bf16 v[88:91], v[220:223], a[152:155], v[88:91]\n" \
  "s_waitcnt vmcnt(19)\n" \
  "v_mfma_f32_16x16x32_bf16 v[92:95], v[220:223], a[156:159], v[92:95]\n" \
  "v_mfma_f32_16x16x32_bf16 v[80:83], v[224:227], a[160:163], v[80:83]\n" \
  "buffer_load_dwordx4 v[220:223], v10, %[rsW], s40 offen offset:1024 sc1\n" \
  "v_mfma_f32_16x16x32_bf16 v[84:87], v[224:227], a[164:167], v[84:87]\n" \
  "v_mfma_f32_16x16x32_bf16 v[88:91], v[224:227], a[168:171], v[88:91]\n" \
  "s_waitcnt vmcnt(19)\n" \
  "v_mfma_f32_16x16x32_bf16 v[92:95], v[224:227], a[172:175], v[92:95]\n" \
  "v_mfma_f32_16x16x32_bf16 v[80:83], v[228:231], a[176:179], v[80:83]\n" \
  "buffer_load_dwordx4 v[224:227], v10, %[rsW], s40 offen offset:2048 sc1\n" \
  "v_mfma_f32_16x16x32_bf16 v[84:87], v[228:231], a[180:183], v[84:87]\n" \
  "v_mfma_f32_16x16x32_bf16 v[88:91], v[228:231], a[184:187], v[88:91]\n" \
  "s_waitcnt vmcnt(19)\n" \
  "v_mfma_f32_16x16x32_bf16 v[92:95], v[228:231], a[188:191], v[92:95]\n" \
  "v_mfma_f32_16x16x32_bf16 v[80:83], v[232:235], a[192:195], v[80:83]\n" \
  "buffer_load_dwordx4 v[228:231], v10, %[rsW], s40 offen offset:3072 sc1\n" \
  "v_mfma_f32_16x16x32_bf16 v[84:87], v[232:235], a[196:199], v[84:87]\n" \
  "v_mfma_f32_16x16x32_bf16 v[88:91], v[232:235], a[200:203], v[88:91]\n" \
  "s_waitcnt vmcnt(19)\n" \
  "v_mfma_f32_16x16x32_bf16 v[92:95], v[232:235], a[204:207], v[92:95]\n" \
  "v_mfma_f32_16x16x32_bf16 v[80:83], v[236:239], a[208:211], v[80:83]\n" \
  "buffer_load_dwordx4 v[232:235], v11, %[rsW], s40 offen sc1\n" \
  "v_mfma_f32_16x16x32_bf16 v[84:87], v[236:239], a[212:215], v[84:87]\n" \
  "v_mfma_f32_16x16x32_bf16 v[88:91], v[236:239], a[216:219], v[88:91]\n" \
  "s_waitcnt vmcnt(19)\n" \
  "v_mfma_f32_16x16x32_bf16 v[92:95], v[236:239], a[220:223], v[92:95]\n" \
  "v_mfma_f32_16x16x32_bf16 v[80:83], v[240:243], a[224:227], v[80:83]\n" \
  "buffer_load_dwordx4 v[236:239], v11, %[rsW], s40 offen offset:1024 sc1\n" \
  "v_mfma_f32_16x16x32_bf16 v[84:87], v[240:243], a[228:231], v[84:87]\n" \
  "v_mfma_f32_16x16x32_bf16 v[88:91], v[240:243], a[232:235], v[88:91]\n" \
  "s_waitcnt vmcnt(19)\n" \
  "v_mfma_f32_16x16x32_bf16 v[92:95], v[240:243], a[236:239], v[92:95]\n" \
  "v_mfma_f32_16x16x32_bf16 v[80:83], v[244:247], a[240:243], v[80:83]\n" \
  "buffer_load_dwordx4 v[240:243], v11, %[rsW], s40 offen offset:2048 sc1\n" \
  "v_mfma_f32_16x16x32_bf16 v[84:87], v[244:247], a[244:247], v[84:87]\n" \
  "v_mfma_f32_16x16x32_bf16 v[88:91], v[244:247], a[248:251], v[88:91]\n" \
  "s_waitcnt vmcnt(19)\n" \
  "v_mfma_f32_16x16x32_bf16 v[92:95], v[244:247], a[252:255], v[92:95]\n" \
  "v_mfma_f32_16x16x32_bf16 v[80:83], v[248:251], v[48:51], v[80:83]\n" \
  "buffer_load_dwordx4 v[244:247], v11, %[rsW], s40 offen offset:3072 sc1\n" \
  "v_mfma_f32_16x16x32_bf16 v[84:87], v[248:251], v[52:55], v[84:87]\n" \
  "v_mfma_f32_16x16x32_bf16 v[88:91], v[248:251], v[56:59], v[88:91]\n" \
  "s_waitcnt vmcnt(19)\n" \
  "v_mfma_f32_16x16x32_bf16 v[92:95], v[248:251], v[60:63], v[92:95]\n" \
  "v_mfma_f32_16x16x32_bf16 v[80:83], v[252:255], v[64:67], v[80:83]\n" \
  "buffer_load_dwordx4 v[248:251], v12, %[rsW], s40 offen sc1\n" \
  "v_mfma_f32_16x16x32_bf16 v[84:87], v[252:255], v[68:71], v[84:87]\n" \
  "v_mfma_f32_16x16x32_bf16 v[88:91], v[252:255], v[72:75], v[88:91]\n" \
  "s_waitcnt vmcnt(19)\n" \
  "v_mfma_f32_16x16x32_bf16 v[92:95], v[252:255], v[76:79], v[92:95]\n" \
  "v_mfma_f32_16x16x32_bf16 v[96:99], v[184:187], a[0:3], 0\n" \
  "buffer_load_dwordx4 v[252:255], v12, %[rsW], s40 offen offset:1024 sc1\n" \
  "v_mfma_f32_16x16x32_bf16 v[100:103], v[184:187], a[4:7], 0\n" \
  "v_mfma_f32_16x16x32_bf16 v[104:107], v[184:187], a[8:11], 0\n" \
  "s_waitcnt vmcnt(19)\n" \
  "v_mfma_f32_16x16x32_bf16 v[108:111], v[184:187], a[12:15], 0\n" \
  "v_mfma_f32_16x16x32_bf16 v[96:99], v[188:191], a[16:19], v[96:99]\n" \
  "s_add_u32 s40, s40, 18432\n" \
  "buffer_load_dwordx4 v[184:187], v8, %[rsW], s40 offen sc1\n" \
  "v_mfma_f32_16x16x32_bf16 v[100:103], v[188:191], a[20:23], v[100:103]\n" \
  "v_mfma_f32_16x16x32_bf16 v[104:107], v[188:191], a[24:27], v[104:107]\n" \
  "s_waitcnt vmcnt(19)\n" \
  "v_mfma_f32_16x16x32_bf16 v[108:111], v[188:191], a[28:31], v[108:111]\n" \
  "v_mfma_f32_16x16x32_bf16 v[96:99], v[192:195], a[32:35], v[96:99]\n" \
  "buffer_load_dwordx4 v[188:191], v8, %[rsW], s40 offen offset:1024 sc1\n" \
  "v_mfma_f32_16x16x32_bf16 v[100:103], v[192:195], a[36:39], v[100:103]\n" \
  "v_cvt_pk_bf16_f32 v30, v80, v81\n" \
  "v_mfma_f32_16x16x32_bf16 v[104:107], v[192:195], a[40:43], v[104:107]\n" \
  "s_waitcnt vmcnt(19)\n" \
  "v_mfma_f32_16x16x32_bf16 v[108:111], v[192:195], a[44:47], v[108:111]\n" \
  "v_cvt_pk_bf16_f32 v31, v82, v83\n" \
  "v_mfma_f32_16x16x32_bf16 v[96:99], v[196:199], a[48:51], v[96:99]\n" \
  "buffer_load_dwordx4 v[192:195], v8, %[rsW], s40 offen offset:2048 sc1\n" \
  "v_mfma_f32_16x16x32_bf16 v[100:103], v[196:199], a[52:55], v[100:103]\n" \
  "v_cvt_pk_bf16_f32 v32, v84, v85\n" \
  "v_mfma_f32_16x16x32_bf16 v[104:107], v[196:199], a[56:59], v[104:107]\n" \
  "s_waitcnt vmcnt(18)\n" \
  "v_mfma_f32_16x16x32_bf16 v[108:111], v[196:199], a[60:63], v[108:111]\n" \
  "v_cvt_pk_bf16_f32 v33, v86, v87\n" \
  "v_mfma_f32_16x16x32_bf16 v[96:99], v[200:203], a[64:67], v[96:99]\n" \
  "buffer_load_dwordx4 v[196:199], v8, %[rsW], s40 offen offset:3072 sc1\n" \
  "v_mfma_f32_16x16x32_bf16 v[100:103], v[200:203], a[68:71], v[100:103]\n" \
  "buffer_store_dwordx4 v[30:33], v8, %[rsP], s42 offen sc1\n" \
  "v_mfma_f32_16x16x32_bf16 v[104:107], v[200:203], a[72:75], v[104:107]\n" \
  "s_waitcnt vmcnt(19)\n" \
  "v_mfma_f32_16x16x32_bf16 v[108:111], v[200:203], a[76:79], v[108:111]\n" \
  "v_cvt_pk_bf16_f32 v34, v88, v89\n" \
  "v_mfma_f32_16x16x32_bf16 v[96:99], v[204:207], a[80:83], v[96:99]\n" \
  "buffer_load_dwordx4 v[200:203], v9, %[rsW], s40 offen sc1\n" \
  "v_mfma_f32_16x16x32_bf16 v[100:103], v[204:207], a[84:87], v[100:103]\n" \
  "v_cvt_pk_bf16_f32 v35, v90, v91\n" \
  "v_mfma_f32_16x16x32_bf16 v[104:107], v[204:207], a[88:91], v[104:107]\n" \
  "s_waitcnt vmcnt(18)\n" \
  "v_mfma_f32_16x16x32_bf16 v[108:111], v[204:207], a[92:95], v[108:111]\n" \
  "v_cvt_pk_bf16_f32 v36, v92, v93\n" \
  "v_mfma_f32_16x16x32_bf16 v[96:99], v[208:211], a[96:99], v[96:99]\n" \
  "buffer_load_dwordx4 v[204:207], v9, %[rsW], s40 offen offset:1024 sc1\n" \
  "v_mfma_f32_16x16x32_bf16 v[100:103], v[208:211], a[100:103], v[100:103]\n" \
  "v_cvt_pk_bf16_f32 v37, v94, v95\n" \
  "v_mfma_f32_16x16x32_bf16 v[104:107], v[208:211], a[104:107], v[104:107]\n" \
  "s_waitcnt vmcnt(17)\n" \
  "v_mfma_f32_16x16x32_bf16 v[108:111], v[208:211], a[108:111], v[108:111]\n" \
  "buffer_store_dwordx4 v[34:37], v8, %[rsP], s42 offen offset:1024 sc1\n" \
  "s_add_u32 s42, s42, 2048\n" \
  "v_mfma_f32_16x16x32_bf16 v[96:99], v[212:215], a[112:115], v[96:99]\n" \
  "buffer_load_dwordx4 v[208:211], v9, %[rsW], s40 offen offset:2048 sc1\n" \
  "v_mfma_f32_16x16x32_bf16 v[100:103], v[212:215], a[116:119], v[100:103]\n" \
  "v_mfma_f32_16x16x32_bf16 v[104:107], v[212:215], a[120:123], v[104:107]\n" \
  "s_waitcnt vmcnt(18)\n" \
  "v_mfma_f32_16x16x32_bf16 v[108:111], v[212:215], a[124:127], v[108:111]\n" \
  "v_mfma_f32_16x16x32_bf16 v[96:99], v[216:219], a[128:131], v[96:99]\n" \
  "buffer_load_dwordx4 v[212:215], v9, %[rsW], s40 offen offset:3072 sc1\n" \
  "v_mfma_f32_16x16x32_bf16 v[100:103], v[216:219], a[132:135], v[100:103]\n" \
  "v_mfma_f32_16x16x32_bf16 v[104:107], v[216:219], a[136:139], v[104:107]\n" \
  "s_waitcnt vmcnt(18)\n" \
  "v_mfma_f32_16x16x32_bf16 v[108:111], v[216:219], a[140:143], v[108:111]\n" \
  "v_mfma_f32_16x16x32_bf16 v[96:99], v[220:223], a[144:147], v[96:99]\n" \
  "buffer_load_dwordx4 v[216:219], v10, %[rsW], s40 offen sc1\n" \
  "v_mfma_f32_16x16x32_bf16 v[100:103], v[220:223], a[148:151], v[100:103]\n" \
  "v_mfma_f32_16x16x32_bf16 v[104:107], v[220:223], a[152:155], v[104:107]\n" \
  "s_waitcnt vmcnt(18)\n" \
  "v_mfma_f32_16x16x32_bf16 v[108:111], v[220:223], a[156:159], v[108:111]\n" \
  "v_mfma_f32_16x16x32_bf16 v[96:99], v[224:227], a[160:163], v[96:99]\n" \
  "buffer_load_dwordx4 v[220:223], v10, %[rsW], s40 offen offset:1024 sc1\n" \
  "v_mfma_f32_16x16x32_bf16 v[100:103], v[224:227], a[164:167], v[100:103]\n" \
  "v_mfma_f32_16x16x32_bf16 v[104:107], v[224:227], a[168:171], v[104:107]\n" \
  "s_waitcnt vmcnt(18)\n" \
  "v_mfma_f32_16x16x32_bf16 v[108:111], v[224:227], a[172:175], v[108:111]\n" \
  "v_mfma_f32_16x16x32_bf16 v[96:99], v[228:231], a[176:179], v[96:99]\n" \
  "buffer_load_dwordx4 v[224:227], v10, %[rsW], s40 offen offset:2048 sc1\n" \
  "v_mfma_f32_16x16x32_bf16 v[100:103], v[228:231], a[180:183], v[100:103]\n" \
  "v_mfma_f32_16x16x32_bf16 v[104:107], v[228:231], a[184:187], v[104:107]\n" \
  "s_waitcnt vmcnt(18)\n" \
  "v_mfma_f32_16x16x32_bf16 v[108:111], v[228:231], a[188:191], v[108:111]\n" \
  "v_mfma_f32_16x16x32_bf16 v[96:99], v[232:235], a[192:195], v[96:99]\n" \
  "buffer_load_dwordx4 v[228:231], v10, %[rsW], s40 offen offset:3072 sc1\n" \
  "v_mfma_f32_16x16x32_bf16 v[100:103], v[232:235], a[196:199], v[100:103]\n" \
  "buffer_load_dword v154, v153, %[rsF], 0 offen sc1\n" \
  "v_mfma_f32_16x16x32_bf16 v[104:107], v[232:235], a[200:203], v[104:107]\n" \
  "s_waitcnt vmcnt(19)\n" \
  "v_mfma_f32_16x16x32_bf16 v[108:111], v[232:235], a[204:207], v[108:111]\n" \
  "v_mfma_f32_16x16x32_bf16 v[96:99], v[236:239], a[208:211], v[96:99]\n" \
  "buffer_load_dwordx4 v[232:235], v11, %[rsW], s40 offen sc1\n" \
  "v_mfma_f32_16x16x32_bf16 v[100:103], v[236:239], a[212:215], v[100:103]\n" \
  "v_mfma_f32_16x16x32_bf16 v[104:107], v[236:239], a[216:219], v[104:107]\n" \
  "s_waitcnt vmcnt(19)\n" \
  "v_mfma_f32_16x16x32_bf16 v[108:111], v[236:239], a[220:223], v[108:111]\n" \
  "v_mfma_f32_16x16x32_bf16 v[96:99], v[240:243], a[224:227], v[96:99]\n" \
  "buffer_load_dwordx4 v[236:239], v11, %[rsW], s40 offen offset:1024 sc1\n" \
  "v_mfma_f32_16x16x32_bf16 v[100:103], v[240:243], a[228:231], v[100:103]\n" \
  "v_mfma_f32_16x16x32_bf16 v[104:107], v[240:243], a[232:235], v[104:107]\n" \
  "s_waitcnt vmcnt(19)\n" \
  "v_mfma_f32_16x16x32_bf16 v[108:111], v[240:243], a[236:239], v[108:111]\n" \
  "v_mfma_f32_16x16x32_bf16 v[96:99], v[244:247], a[240:243], v[96:99]\n" \
  "buffer_load_dwordx4 v[240:243], v11, %[rsW], s40 offen offset:2048 sc1\n" \
  "v_mfma_f32_16x16x32_bf16 v[100:103], v[244:247], a[244:247], v[100:103]\n" \
  "v_mfma_f32_16x16x32_bf16 v[104:107], v[244:247], a[248:251], v[104:107]\n" \
  "s_waitcnt vmcnt(19)\n" \
  "v_mfma_f32_16x16x32_bf16 v[108:111], v[244:247], a[252:255], v[108:111]\n" \
  "v_mfma_f32_16x16x32_bf16 v[96:99], v[248:251], v[48:51], v[96:99]\n" \
  "buffer_load_dwordx4 v[244:247], v11, %[rsW], s40 offen offset:3072 sc1\n" \
  "v_mfma_f32_16x16x32_bf16 v[100:103], v[248:251], v[52:55], v[100:103]\n" \
  "v_mfma_f32_16x16x32_bf16 v[104:107], v[248:251], v[56:59], v[104:107]\n" \
  "s_waitcnt vmcnt(19)\n" \
  "v_mfma_f32_16x16x32_bf16 v[108:111], v[248:251], v[60:63], v[108:111]\n" \
  "v_mfma_f32_16x16x32_bf16 v[96:99], v[252:255], v[64:67], v[96:99]\n" \
  "buffer_load_dwordx4 v[248:251], v12, %[rsW], s40 offen sc1\n" \
  "v_mfma_f32_16x16x32_bf16 v[100:103], v[252:255], v[68:71], v[100:103]\n" \
  "v_mfma_f32_16x16x32_bf16 v[104:107], v[252:255], v[72:75], v[104:107]\n" \
  "s_waitcnt vmcnt(19)\n" \
  "v_mfma_f32_16x16x32_bf16 v[108:111], v[252:255], v[76:79], v[108:111]\n" \
  "v_mfma_f32_16x16x32_bf16 v[80:83], v[184:187], a[0:3], 0\n" \
  "buffer_load_dwordx4 v[252:255], v12, %[rsW], s40 offen offset:1024 sc1\n" \
  "v_mfma_f32_16x16x32_bf16 v[84:87], v[184:187], a[4:7], 0\n" \
  "v_mfma_f32_16x16x32_bf16 v[88:91], v[184:187], a[8:11], 0\n" \
  "s_waitcnt vmcnt(19)\n" \
  "v_mfma_f32_16x16x32_bf16 v[92:95], v[184:187], a[12:15], 0\n" \
  "v_mfma_f32_16x16x32_bf16 v[80:83], v[188:191], a[16:19], v[80:83]\n" \
  "s_add_u32 s40, s40, 18432\n" \
  "buffer_load_dwordx4 v[184:187], v8, %[rsW], s40 offen sc1\n" \
  "v_mfma_f32_16x16x32_bf16 v[84:87], v[188:191], a[20:23], v[84:87]\n" \
  "v_mfma_f32_16x16x32_bf16 v[88:91], v[188:191], a[24:27], v[88:91]\n" \
  "s_waitcnt vmcnt(19)\n" \
  "v_mfma_f32_16x16x32_bf16 v[92:95], v[188:191], a[28:31], v[92:95]\n" \
  "v_mfma_f32_16x16x32_bf16 v[80:83], v[192:195], a[32:35], v[80:83]\n" \
  "buffer_load_dwordx4 v[188:191], v8, %[rsW], s40 offen offset:1024 sc1\n" \
  "v_mfma_f32_16x16x32_bf16 v[84:87], v[192:195], a[36:39], v[84:87]\n" \
  "v_cvt_pk_bf16_f32 v30, v96, v97\n" \
  "v_mfma_f32_16x16x32_bf16 v[88:91], v[192:195], a[40:43], v[88:91]\n" \
  "s_waitcnt vmcnt(19)\n" \
  "v_mfma_f32_16x16x32_bf16 v[92:95], v[192:195], a[44:47], v[92:95]\n" \
  "v_cvt_pk_bf16_f32 v31, v98, v99\n" \
  "v_mfma_f32_16x16x32_bf16 v[80:83], v[196:199], a[48:51], v[80:83]\n" \
  "buffer_load_dwordx4 v[192:195], v8, %[rsW], s40 offen offset:2048 sc1\n" \
  "v_mfma_f32_16x16x32_bf16 v[84:87], v[196:199], a[52:55], v[84:87]\n" \
  "v_cvt_pk_bf16_f32 v32, v100, v101\n" \
  "v_mfma_f32_16x16x32_bf16 v[88:91], v[196:199], a[56:59], v[88:91]\n" \
  "s_waitcnt vmcnt(18)\n" \
  "v_mfma_f32_16x16x32_bf16 v[92:95], v[196:199], a[60:63], v[92:95]\n" \
  "v_cvt_pk_bf16_f32 v33, v102, v103\n" \
  "v_mfma_f32_16x16x32_bf16 v[80:83], v[200:203], a[64:67], v[80:83]\n" \
  "buffer_load_dwordx4 v[196:199], v8, %[rsW], s40 offen offset:3072 sc1\n" \
  "v_mfma_f32_16x16x32_bf16 v[84:87], v[200:203], a[68:71], v[84:87]\n" \
  "buffer_store_dwordx4 v[30:33], v8, %[rsP], s42 offen sc1\n" \
  "v_mfma_f32_16x16x32_bf16 v[88:91], v[200:203], a[72:75], v[88:91]\n" \
  "s_waitcnt vmcnt(19)\n" \
  "v_mfma_f32_16x16x32_bf16 v[92:95], v[200:203], a[76:79], v[92:95]\n" \
  "v_cvt_pk_bf16_f32 v34, v104, v105\n" \
  "v_mfma_f32_16x16x32_bf16 v[80:83], v[204:207], a[80:83], v[80:83]\n" \
  "buffer_load_dwordx4 v[200:203], v9, %[rsW], s40 offen sc1\n" \
  "v_mfma_f32_16x16x32_bf16 v[84:87], v[204:207], a[84:87], v[84:87]\n" \
  "v_cvt_pk_bf16_f32 v35, v106, v107\n" \
  "v_mfma_f32_16x16x32_bf16 v[88:91], v[204:207], a[88:91], v[88:91]\n" \
  "s_waitcnt vmcnt(18)\n" \
  "v_mfma_f32_16x16x32_bf16 v[92:95], v[204:207], a[92:95], v[92:95]\n" \
  "v_cvt_pk_bf16_f32 v36, v108, v109\n" \
  "v_mfma_f32_16x16x32_bf16 v[80:83], v[208:211], a[96:99], v[80:83]\n" \
  "buffer_load_dwordx4 v[204:207], v9, %[rsW], s40 offen offset:1024 sc1\n" \
  "v_mfma_f32_16x16x32_bf16 v[84:87], v[208:211], a[100:103], v[84:87]\n" \
  "v_cvt_pk_bf16_f32 v37, v110, v111\n" \
  "v_mfma_f32_16x16x32_bf16 v[88:91], v[208:211], a[104:107], v[88:91]\n" \
  "s_waitcnt vmcnt(18)\n" \
  "v_mfma_f32_16x16x32_bf16 v[92:95], v[208:211], a[108:111], v[92:95]\n" \
  "buffer_store_dwordx4 v[34:37], v8, %[rsP], s42 offen offset:1024 sc1\n" \
  "s_add_u32 s42, s42, 2048\n" \
  "v_mfma_f32_16x16x32_bf16 v[80:83], v[212:215], a[112:115], v[80:83]\n" \
  "buffer_load_dwordx4 v[208:211], v9, %[rsW], s40 offen offset:2048 sc1\n" \
  "v_mfma_f32_16x16x32_bf16 v[84:87], v[212:215], a[116:119], v[84:87]\n" \
  "v_mfma_f32_16x16x32_bf16 v[88:91], v[212:215], a[120:123], v[88:91]\n" \
  "s_waitcnt vmcnt(19)\n" \
  "v_mfma_f32_16x16x32_bf16 v[92:95], v[212:215], a[124:127], v[92:95]\n" \
  "v_mfma_f32_16x16x32_bf16 v[80:83], v[216:219], a[128:131], v[80:83]\n" \
  "buffer_load_dwordx4 v[212:215], v9, %[rsW], s40 offen offset:3072 sc1\n" \
  "v_mfma_f32_16x16x32_bf16 v[84:87], v[216:219], a[132:135], v[84:87]\n" \
  "v_mfma_f32_16x16x32_bf16 v[88:91], v[216:219], a[136:139], v[88:91]\n" \
  "s_waitcnt vmcnt(19)\n" \
  "v_mfma_f32_16x16x32_bf16 v[92:95], v[216:219], a[140:143], v[92:95]\n" \
  "v_mfma_f32_16x16x32_bf16 v[80:83], v[220:223], a[144:147], v[80:83]\n" \
  "buffer_load_dwordx4 v[216:219], v10, %[rsW], s40 offen sc1\n" \
  "v_mfma_f32_16x16x32_bf16 v[84:87], v[220:223], a[148:151], v[84:87]\n" \
  "v_mfma_f32_16x16x32_bf16 v[88:91], v[220:223], a[152:155], v[88:91]\n" \
  "s_waitcnt vmcnt(19)\n" \
  "v_mfma_f32_16x16x32_bf16 v[92:95], v[220:223], a[156:159], v[92:95]\n" \
  "v_mfma_f32_16x16x32_bf16 v[80:83], v[224:227], a[160:163], v[80:83]\n" \
  "buffer_load_dwordx4 v[220:223], v10, %[rsW], s40 offen offset:1024 sc1\n" \
  "v_mfma_f32_16x16x32_bf16 v[84:87], v[224:227], a[164:167], v[84:87]\n" \
  "v_mfma_f32_16x16x32_bf16 v[88:91], v[224:227], a[168:171], v[88:91]\n" \
  "s_waitcnt vmcnt(19)\n" \
  "v_mfma_f32_16x16x32_bf16 v[92:95], v[224:227], a[172:175], v[92:95]\n" \
  "v_mfma_f32_16x16x32_bf16 v[80:83], v[228:231], a[176:179], v[80:83]\n" \
  "buffer_load_dwordx4 v[224:227], v10, %[rsW], s40 offen offset:2048 sc1\n" \
  "v_mfma_f32_16x16x32_bf16 v[84:87], v[228:231], a[180:183], v[84:87]\n" \
  "v_mfma_f32_16x16x32_bf16 v[88:91], v[228:231], a[184:187], v[88:91]\n" \
  "s_waitcnt vmcnt(18)\n" \
  "v_mfma_f32_16x16x32_bf16 v[92:95], v[228:231], a[188:191], v[92:95]\n" \
  "v_readfirstlane_b32 s52, v154\n" \
  "s_nop 3\n" \
  "s_cmp_ge_u32 s52, 32\n" \
  "s_cbranch_scc0 L_spinA_%=\n" \
  "L_backA_%=:\n" \
  "v_mfma_f32_16x16x32_bf16 v[80:83], v[232:235], a[192:195], v[80:83]\n" \
  "buffer_load_dwordx4 v[228:231], v10, %[rsW], s40 offen offset:3072 sc1\n" \
  "v_mfma_f32_16x16x32_bf16 v[84:87], v[232:235], a[196:199], v[84:87]\n" \
  "buffer_load_dwordx4 %[pa0], v8, %[rsA], s53 offen sc1\n" \
  "s_add_u32 s53, s53, 2359296\n" \
  "v_mfma_f32_16x16x32_bf16 v[88:91], v[232:235], a[200:203], v[88:91]\n" \
  "s_waitcnt vmcnt(19)\n" \
  "v_mfma_f32_16x16x32_bf16 v[92:95], v[232:235], a[204:207], v[92:95]\n" \
  "v_mfma_f32_16x16x32_bf16 v[80:83], v[236:239], a[208:211], v[80:83]\n" \
  "buffer_load_dwordx4 v[232:235], v11, %[rsW], s40 offen sc1\n" \
  "v_mfma_f32_16x16x32_bf16 v[84:87], v[236:239], a[212:215], v[84:87]\n" \
  "v_mfma_f32_16x16x32_bf16 v[88:91], v[236:239], a[216:219], v[88:91]\n" \
  "s_waitcnt vmcnt(19)\n" \
  "v_mfma_f32_16x16x32_bf16 v[92:95], v[236:239], a[220:223], v[92:95]\n" \
  "v_mfma_f32_16x16x32_bf16 v[80:83], v[240:243], a[224:227], v[80:83]\n" \
  "buffer_load_dwordx4 v[236:239], v11, %[rsW], s40 offen offset:1024 sc1\n" \
  "v_mfma_f32_16x16x32_bf16 v[84:87], v[240:243], a[228:231], v[84:87]\n" \
  "buffer_load_dwordx4 %[pa1], v8, %[rsA], s53 offen sc1\n" \
  "s_add_u32 s53, s53, 2359296\n" \
  "v_mfma_f32_16x16x32_bf16 v[88:91], v[240:243], a[232:235], v[88:91]\n" \
  "s_waitcnt vmcnt(20)\n" \
  "v_mfma_f32_16x16x32_bf16 v[92:95], v[240:243], a[236:239], v[92:95]\n" \
  "v_mfma_f32_16x16x32_bf16 v[80:83], v[244:247], a[240:243], v[80:83]\n" \
  "buffer_load_dwordx4 v[240:243], v11, %[rsW], s40 offen offset:2048 sc1\n" \
  "v_mfma_f32_16x16x32_bf16 v[84:87], v[244:247], a[244:247], v[84:87]\n" \
  "v_mfma_f32_16x16x32_bf16 v[88:91], v[244:247], a[248:251], v[88:91]\n" \
  "s_waitcnt vmcnt(20)\n" \
  "v_mfma_f32_16x16x32_bf16 v[92:95], v[244:247], a[252:255], v[92:95]\n" \
  "v_mfma_f32_16x16x32_bf16 v[80:83], v[248:251], v[48:51], v[80:83]\n" \
  "buffer_load_dwordx4 v[244:247], v11, %[rsW], s40 offen offset:3072 sc1\n" \
  "v_mfma_f32_16x16x32_bf16 v[84:87], v[248:251], v[52:55], v[84:87]\n" \
  "buffer_load_dwordx4 %[pa2], v8, %[rsA], s53 offen sc1\n" \
  "s_add_u32 s53, s53, 2359296\n" \
  "v_mfma_f32_16x16x32_bf16 v[88:91], v[248:251], v[56:59], v[88:91]\n" \
  "s_waitcnt vmcnt(21)\n" \
  "v_mfma_f32_16x16x32_bf16 v[92:95], v[248:251], v[60:63], v[92:95]\n" \
  "s_waitcnt vmcnt(13)\n" \
  "s_mov_b32 s52, 4096\n" \
  "s_mov_b64 exec, 1\n" \
  "buffer_atomic_add v152, v153, %[rsF], s52 offen\n" \
  "s_mov_b64 exec, -1\n" \
  "s_nop 3\n" \
  "v_mfma_f32_16x16x32_bf16 v[80:83], v[252:255], v[64:67], v[80:83]\n" \
  "buffer_load_dwordx4 v[248:251], v12, %[rsW], s40 offen sc1\n" \
  "v_mfma_f32_16x16x32_bf16 v[84:87], v[252:255], v[68:71], v[84:87]\n" \
  "v_mfma_f32_16x16x32_bf16 v[88:91], v[252:255], v[72:75], v[88:91]\n" \
  "v_mfma_f32_16x16x32_bf16 v[92:95], v[252:255], v[76:79], v[92:95]\n" \
  "v_mfma_f32_16x16x32_bf16 v[96:99], v[184:187], a[0:3], 0\n" \
  "buffer_load_dwordx4 v[252:255], v12, %[rsW], s40 offen offset:1024 sc1\n" \
  "v_mfma_f32_16x16x32_bf16 v[100:103], v[184:187], a[4:7], 0\n" \
  "buffer_load_dwordx4 %[pa3], v8, %[rsA], s53 offen sc1\n" \
  "s_add_u32 s53, s53, 2359296\n" \
  "v_mfma_f32_16x16x32_bf16 v[104:107], v[184:187], a[8:11], 0\n" \
  "v_mfma_f32_16x16x32_bf16 v[108:111], v[184:187], a[12:15], 0\n" \
  "v_mfma_f32_16x16x32_bf16 v[96:99], v[188:191], a[16:19], v[96:99]\n" \
  "v_mfma_f32_16x16x32_bf16 v[100:103], v[188:191], a[20:23], v[100:103]\n" \
  "v_mfma_f32_16x16x32_bf16 v[104:107], v[188:191], a[24:27], v[104:107]\n" \
  "v_mfma_f32_16x16x32_bf16 v[108:111], v[188:191], a[28:31], v[108:111]\n" \
  "v_mfma_f32_16x16x32_bf16 v[96:99], v[192:195], a[32:35], v[96:99]\n" \
  "v_mfma_f32_16x16x32_bf16 v[100:103], v[192:195], a[36:39], v[100:103]\n" \
  "v_cvt_pk_bf16_f32 v30, v80, v81\n" \
  "buffer_load_dwordx4 %[pa4], v8, %[rsA], s53 offen sc1\n" \
  "s_add_u32 s53, s53, 2359296\n" \
  "v_mfma_f32_16x16x32_bf16 v[104:107], v[192:195], a[40:43], v[104:107]\n" \
  "v_mfma_f32_16x16x32_bf16 v[108:111], v[192:195], a[44:47], v[108:111]\n" \
  "v_cvt_pk_bf16_f32 v31, v82, v83\n" \
  "v_mfma_f32_16x16x32_bf16 v[96:99], v[196:199], a[48:51], v[96:99]\n" \
  "v_mfma_f32_16x16x32_bf16 v[100:103], v[196:199], a[52:55], v[100:103]\n" \
  "v_cvt_pk_bf16_f32 v32, v84, v85\n" \
  "v_mfma_f32_16x16x32_bf16 v[104:107], v[196:199], a[56:59], v[104:107]\n" \
  "v_mfma_f32_16x16x32_bf16 v[108:111], v[196:199], a[60:63], v[108:111]\n" \
  "v_cvt_pk_bf16_f32 v33, v86, v87\n" \
  "v_mfma_f32_16x16x32_bf16 v[96:99], v[200:203], a[64:67], v[96:99]\n" \
  "v_mfma_f32_16x16x32_bf16 v[100:103], v[200:203], a[68:71], v[100:103]\n" \
  "buffer_store_dwordx4 v[30:33], v8, %[rsP], s42 offen sc1\n" \
  "buffer_load_dwordx4 %[pa5], v8, %[rsA], s53 offen sc1\n" \
  "s_add_u32 s53, s53, 2359296\n" \
  "v_mfma_f32_16x16x32_bf16 v[104:107], v[200:203], a[72:75], v[104:107]\n" \
  "v_mfma_f32_16x16x32_bf16 v[108:111], v[200:203], a[76:79], v[108:111]\n" \
  "v_cvt_pk_bf16_f32 v34, v88, v89\n" \
  "v_mfma_f32_16x16x32_bf16 v[96:99], v[204:207], a[80:83], v[96:99]\n" \
  "v_mfma_f32_16x16x32_bf16 v[100:103], v[204:207], a[84:87], v[100:103]\n" \
  "v_cvt_pk_bf16_f32 v35, v90, v91\n" \
  "v_mfma_f32_16x16x32_bf16 v[104:107], v[204:207], a[88:91], v[104:107]\n" \
  "s_waitcnt vmcnt(19)\n" \
  "v_mfma_f32_16x16x32_bf16 v[108:111], v[204:207], a[92:95], v[108:111]\n" \
  "v_cvt_pk_bf16_f32 v36, v92, v93\n" \
  "v_mfma_f32_16x16x32_bf16 v[96:99], v[208:211], a[96:99], v[96:99]\n" \
  "v_mfma_f32_16x16x32_bf16 v[100:103], v[208:211], a[100:103], v[100:103]\n" \
  "v_cvt_pk_bf16_f32 v37, v94, v95\n" \
  "buffer_load_dwordx4 %[pa6], v8, %[rsA], s53 offen sc1\n" \
  "s_add_u32 s53, s53, 2359296\n" \
  "v_mfma_f32_16x16x32_bf16 v[104:107], v[208:211], a[104:107], v[104:107]\n" \
  "s_waitcnt vmcnt(19)\n" \
  "v_mfma_f32_16x16x32_bf16 v[108:111], v[208:211], a[108:111], v[108:111]\n" \
  "buffer_store_dwordx4 v[34:37], v8, %[rsP], s42 offen offset:1024 sc1\n" \
  "s_add_u32 s42, s42, 2048\n" \
  "v_mfma_f32_16x16x32_bf16 v[96:99], v[212:215], a[112:115], v[96:99]\n" \
  "v_mfma_f32_16x16x32_bf16 v[100:103], v[212:215], a[116:119], v[100:103]\n" \
  "v_mfma_f32_16x16x32_bf16 v[104:107], v[212:215], a[120:123], v[104:107]\n" \
  "s_waitcnt vmcnt(19)\n" \
  "v_mfma_f32_16x16x32_bf16 v[108:111], v[212:215], a[124:127], v[108:111]\n" \
  "v_mfma_f32_16x16x32_bf16 v[96:99], v[216:219], a[128:131], v[96:99]\n" \
  "v_mfma_f32_16x16x32_bf16 v[100:103], v[216:219], a[132:135], v[100:103]\n" \
  "buffer_load_dwordx4 %[pa7], v8, %[rsA], s53 offen sc1\n" \
  "s_add_u32 s53, s53, 2359296\n" \
  "v_mfma_f32_16x16x32_bf16 v[104:107], v[216:219], a[136:139], v[104:107]\n" \
  "s_waitcnt vmcnt(19)\n" \
  "v_mfma_f32_16x16x32_bf16 v[108:111], v[216:219], a[140:143], v[108:111]\n" \
  "v_mfma_f32_16x16x32_bf16 v[96:99], v[220:223], a[144:147], v[96:99]\n" \
  "v_mfma_f32_16x16x32_bf16 v[100:103], v[220:223], a[148:151], v[100:103]\n" \
  "v_mfma_f32_16x16x32_bf16 v[104:107], v[220:223], a[152:155], v[104:107]\n" \
  "s_waitcnt vmcnt(18)\n" \
  "v_mfma_f32_16x16x32_bf16 v[108:111], v[220:223], a[156:159], v[108:111]\n" \
  "v_mfma_f32_16x16x32_bf16 v[96:99], v[224:227], a[160:163], v[96:99]\n" \
  "v_mfma_f32_16x16x32_bf16 v[100:103], v[224:227], a[164:167], v[100:103]\n" \
  "v_mfma_f32_16x16x32_bf16 v[104:107], v[224:227], a[168:171], v[104:107]\n" \
  "s_waitcnt vmcnt(17)\n" \
  "v_mfma_f32_16x16x32_bf16 v[108:111], v[224:227], a[172:175], v[108:111]\n" \
  "v_mfma_f32_16x16x32_bf16 v[96:99], v[228:231], a[176:179], v[96:99]\n" \
  "v_mfma_f32_16x16x32_bf16 v[100:103], v[228:231], a[180:183], v[100:103]\n" \
  "v_mfma_f32_16x16x32_bf16 v[104:107], v[228:231], a[184:187], v[104:107]\n" \
  "s_waitcnt vmcnt(15)\n" \
  "v_mfma_f32_16x16x32_bf16 v[108:111], v[228:231], a[188:191], v[108:111]\n" \
  "v_mfma_f32_16x16x32_bf16 v[96:99], v[232:235], a[192:195], v[96:99]\n" \
  "v_mfma_f32_16x16x32_bf16 v[100:103], v[232:235], a[196:199], v[100:103]\n" \
  "v_mfma_f32_16x16x32_bf16 v[104:107], v[232:235], a[200:203], v[104:107]\n" \
  "s_waitcnt vmcnt(14)\n" \
  "v_mfma_f32_16x16x32_bf16 v[108:111], v[232:235], a[204:207], v[108:111]\n" \
  "v_mfma_f32_16x16x32_bf16 v[96:99], v[236:239], a[208:211], v[96:99]\n" \
  "v_mfma_f32_16x16x32_bf16 v[100:103], v[236:239], a[212:215], v[100:103]\n" \
  "v_mfma_f32_16x16x32_bf16 v[104:107], v[236:239], a[216:219], v[104:107]\n" \
  "s_waitcnt vmcnt(12)\n" \
  "v_mfma_f32_16x16x32_bf16 v[108:111], v[236:239], a[220:223], v[108:111]\n" \
  "v_mfma_f32_16x16x32_bf16 v[96:99], v[240:243], a[224:227], v[96:99]\n" \
  "v_mfma_f32_16x16x32_bf16 v[100:103], v[240:243], a[228:231], v[100:103]\n" \
  "v_mfma_f32_16x16x32_bf16 v[104:107], v[240:243], a[232:235], v[104:107]\n" \
  "s_waitcnt vmcnt(11)\n" \
  "v_mfma_f32_16x16x32_bf16 v[108:111], v[240:243], a[236:239], v[108:111]\n" \
  "v_mfma_f32_16x16x32_bf16 v[96:99], v[244:247], a[240:243], v[96:99]\n" \
  "v_mfma_f32_16x16x32_bf16 v[100:103], v[244:247], a[244:247], v[100:103]\n" \
  "v_mfma_f32_16x16x32_bf16 v[104:107], v[244:247], a[248:251], v[104:107]\n" \
  "s_waitcnt vmcnt(8)\n" \
  "v_mfma_f32_16x16x32_bf16 v[108:111], v[244:247], a[252:255], v[108:111]\n" \
  "v_mfma_f32_16x16x32_bf16 v[96:99], v[248:251], v[48:51], v[96:99]\n" \
  "v_mfma_f32_16x16x32_bf16 v[100:103], v[248:251], v[52:55], v[100:103]\n" \
  "v_mfma_f32_16x16x32_bf16 v[104:107], v[248:251], v[56:59], v[104:107]\n" \
  "s_waitcnt vmcnt(7)\n" \
  "v_mfma_f32_16x16x32_bf16 v[108:111], v[248:251], v[60:63], v[108:111]\n" \
  "v_mfma_f32_16x16x32_bf16 v[96:99], v[252:255], v[64:67], v[96:99]\n" \
  "v_mfma_f32_16x16x32_bf16 v[100:103], v[252:255], v[68:71], v[100:103]\n" \
  "v_mfma_f32_16x16x32_bf16 v[104:107], v[252:255], v[72:75], v[104:107]\n" \
  "v_mfma_f32_16x16x32_bf16 v[108:111], v[252:255], v[76:79], v[108:111]\n" \
  "s_nop 7\n" \
  "v_cvt_pk_bf16_f32 v30, v96, v97\n" \
  "v_cvt_pk_bf16_f32 v31, v98, v99\n" \
  "v_cvt_pk_bf16_f32 v32, v100, v101\n" \
  "v_cvt_pk_bf16_f32 v33, v102, v103\n" \
  "buffer_store_dwordx4 v[30:33], v8, %[rsP], s42 offen sc1\n" \
  "v_cvt_pk_bf16_f32 v34, v104, v105\n" \
  "v_cvt_pk_bf16_f32 v35, v106, v107\n" \
  "v_cvt_pk_bf16_f32 v36, v108, v109\n" \
  "v_cvt_pk_bf16_f32 v37, v110, v111\n" \
  "buffer_store_dwordx4 v[34:37], v8, %[rsP], s42 offen offset:1024 sc1\n" \
  "s_add_u32 s42, s42, 2048\n" \
  "s_mov_b32 s52, 4096\n" \
  "buffer_load_dword %[pB], v153, %[rsF], s52 offen sc1\n" \
  "s_waitcnt vmcnt(1)\n" \
  "s_mov_b32 s52, 8192\n" \
  "s_mov_b64 exec, 1\n" \
  "buffer_atomic_add v152, v153, %[rsF], s52 offen\n" \
  "s_mov_b64 exec, -1\n" \
  "s_nop 3\n" \
  "s_waitcnt vmcnt(1)\n" \
  "s_branch L_done_%=\n" \
  "L_spinA_%=:\n" \
  "s_mov_b32 s54, 0x40000\n" \
  "L_spinL_%=:\n" \
  "buffer_load_dword v154, v153, %[rsF], 0 offen sc1\n" \
  "s_waitcnt vmcnt(0)\n" \
  "v_readfirstlane_b32 s52, v154\n" \
  "s_nop 3\n" \
  "s_cmp_ge_u32 s52, 32\n" \
  "s_cbranch_scc1 L_backA_%=\n" \
  "s_sub_u32 s54, s54, 1\n" \
  "s_cmp_lg_u32 s54, 0\n" \
  "s_cbranch_scc1 L_spinL_%=\n" \
  "s_branch L_backA_%=\n" \
  "L_done_%=:\n" \
  ""
#define K3_CLOB "v8","v9","v10","v11","v12","v13","v14","v15","v16","v17","v18","v19","v20","v21","v22","v23","v24","v25","v26","v27","v28","v29","v30","v31","v32","v33","v34","v35","v36","v37","v38","v39","v40","v41","v42","v43","v44","v45","v46","v47","v48","v49","v50","v51","v52","v53","v54","v55","v56","v57","v58","v59","v60","v61","v62","v63","v64","v65","v66","v67","v68","v69","v70","v71","v72","v73","v74","v75","v76","v77","v78","v79","v80","v81","v82","v83","v84","v85","v86","v87","v88","v89","v90","v91","v92","v93","v94","v95","v96","v97","v98","v99","v100","v101","v102","v103","v104","v105","v106","v107","v108","v109","v110","v111","v152","v153","v154","v155","v156","v157","v158","v159","v160","v161","v162","v163","v164","v165","v166","v167","v168","v169","v170","v171","v172","v173","v174","v175","v176","v177","v178","v179","v180","v181","v182","v183","v184","v185","v186","v187","v188","v189","v190","v191","v192","v193","v194","v195","v196","v197","v198","v199","v200","v201","v202","v203","v204","v205","v206","v207","v208","v209","v210","v211","v212","v213","v214","v215","v216","v217","v218","v219","v220","v221","v222","v223","v224","v225","v226","v227","v228","v229","v230","v231","v232","v233","v234","v235","v236","v237","v238","v239","v240","v241","v242","v243","v244","v245","v246","v247","v248","v249","v250","v251","v252","v253","v254","v255","a0","a1","a2","a3","a4","a5","a6","a7","a8","a9","a10","a11","a12","a13","a14","a15","a16","a17","a18","a19","a20","a21","a22","a23","a24","a25","a26","a27","a28","a29","a30","a31","a32","a33","a34","a35","a36","a37","a38","a39","a40","a41","a42","a43","a44","a45","a46","a47","a48","a49","a50","a51","a52","a53","a54","a55","a56","a57","a58","a59","a60","a61","a62","a63","a64","a65","a66","a67","a68","a69","a70","a71","a72","a73","a74","a75","a76","a77","a78","a79","a80","a81","a82","a83","a84","a85","a86","a87","a88","a89","a90","a91","a92","a93","a94","a95","a96","a97","a98","a99","a100","a101","a102","a103","a104","a105","a106","a107","a108","a109","a110","a111","a112","a113","a114","a115","a116","a117","a118","a119","a120","a121","a122","a123","a124","a125","a126","a127","a128","a129","a130","a131","a132","a133","a134","a135","a136","a137","a138","a139","a140","a141","a142","a143","a144","a145","a146","a147","a148","a149","a150","a151","a152","a153","a154","a155","a156","a157","a158","a159","a160","a161","a162","a163","a164","a165","a166","a167","a168","a169","a170","a171","a172","a173","a174","a175","a176","a177","a178","a179","a180","a181","a182","a183","a184","a185","a186","a187","a188","a189","a190","a191","a192","a193","a194","a195","a196","a197","a198","a199","a200","a201","a202","a203","a204","a205","a206","a207","a208","a209","a210","a211","a212","a213","a214","a215","a216","a217","a218","a219","a220","a221","a222","a223","a224","a225","a226","a227","a228","a229","a230","a231","a232","a233","a234","a235","a236","a237","a238","a239","a240","a241","a242","a243","a244","a245","a246","a247","a248","a249","a250","a251","a252","a253","a254","a255","s40","s41","s42","s43","s44","s45","s46","s47","s48","s49","s50","s51","s52","s53","s54","s55","s56","s57","s58","s59","s60","s61","s62","s63","s64","s65","s66","s67","s68","s69","s70","s71","s72","s73","s74","s75","scc","vcc","m0","exec","memory"
#define K3_LDS 153088
#define K2_ASM \
  "ds_read_b64 v[64:65], %[vb] offset:240\n" \
  "ds_read_b64 v[66:67], %[vb] offset:272\n" \
  "ds_read_b64 v[68:69], %[vb] offset:1520\n" \
  "ds_read_b64 v[70:71], %[vb] offset:1552\n" \
  "ds_read_b64 v[72:73], %[vb] offset:2800\n" \
  "ds_read_b64 v[74:75], %[vb] offset:2832\n" \
  "ds_read_b64 v[76:77], %[vb] offset:4080\n" \
  "ds_read_b64 v[78:79], %[vb] offset:4112\n" \
  "ds_read_b64 v[96:97], %[va] offset:3296\n" \
  "ds_read_b64 v[98:99], %[va] offset:3304\n" \
  "ds_read_b64 v[100:101], %[va] offset:3264\n" \
  "ds_read_b64 v[102:103], %[va] offset:3272\n" \
  "ds_read_b64 v[104:105], %[va] offset:2272\n" \
  "ds_read_b64 v[106:107], %[va] offset:2280\n" \
  "s_waitcnt lgkmcnt(4)\n" \
  "v_mfma_f32_16x16x32_f16 %[c00], v[96:99], v[64:67], 0\n" \
  "ds_read_b64 v[108:109], %[va] offset:2240\n" \
  "ds_read_b64 v[110:111], %[va] offset:2248\n" \
  "v_mfma_f32_16x16x32_f16 %[c10], v[96:99], v[68:71], 0\n" \
  "ds_read_b64 v[112:113], %[va] offset:1248\n" \
  "ds_read_b64 v[114:115], %[va] offset:1256\n" \
  "v_mfma_f32_16x16x32_f16 %[c20], v[96:99], v[72:75], 0\n" \
  "v_mfma_f32_16x16x32_f16 %[c30], v[96:99], v[76:79], 0\n" \
  "s_waitcnt lgkmcnt(6)\n" \
  "v_mfma_f32_16x16x32_f16 %[c01], v[100:103], v[64:67], 0\n" \
  "ds_read_b64 v[116:117], %[va] offset:1216\n" \
  "ds_read_b64 v[118:119], %[va] offset:1224\n" \
  "v_mfma_f32_16x16x32_f16 %[c11], v[100:103], v[68:71], 0\n" \
  "v_mfma_f32_16x16x32_f16 %[c21], v[100:103], v[72:75], 0\n" \
  "v_mfma_f32_16x16x32_f16 %[c31], v[100:103], v[76:79], 0\n" \
  "s_waitcnt lgkmcnt(6)\n" \
  "v_mfma_f32_16x16x32_f16 %[c10], v[104:107], v[64:67], %[c10]\n" \
  "ds_read_b64 v[96:97], %[va] offset:224\n" \
  "ds_read_b64 v[98:99], %[va] offset:232\n" \
  "v_mfma_f32_16x16x32_f16 %[c20], v[104:107], v[68:71], %[c20]\n" \
  "ds_read_b64 v[80:81], %[vb] offset:160\n" \
  "ds_read_b64 v[82:83], %[vb] offset:192\n" \
  "v_mfma_f32_16x16x32_f16 %[c30], v[104:107], v[72:75], %[c30]\n" \
  "ds_read_b64 v[84:85], %[vb] offset:1440\n" \
  "ds_read_b64 v[86:87], %[vb] offset:1472\n" \
  "s_waitcnt lgkmcnt(10)\n" \
  "v_mfma_f32_16x16x32_f16 %[c11], v[108:111], v[64:67], %[c11]\n" \
  "ds_read_b64 v[100:101], %[va] offset:192\n" \
  "ds_read_b64 v[102:103], %[va] offset:200\n" \
  "v_mfma_f32_16x16x32_f16 %[c21], v[108:111], v[68:71], %[c21]\n" \
  "ds_read_b64 v[88:89], %[vb] offset:2720\n" \
  "ds_read_b64 v[90:91], %[vb] offset:2752\n" \
  "v_mfma_f32_16x16x32_f16 %[c31], v[108:111], v[72:75], %[c31]\n" \
  "ds_read_b64 v[92:93], %[vb] offset:4000\n" \
  "s_waitcnt lgkmcnt(14)\n" \
  "ds_read_b64 v[94:95], %[vb] offset:4032\n" \
  "s_waitcnt lgkmcnt(14)\n" \
  "v_mfma_f32_16x16x32_f16 %[c20], v[112:115], v[64:67], %[c20]\n" \
  "ds_read_b64 v[104:105], %[va] offset:3232\n" \
  "s_waitcnt lgkmcnt(14)\n" \
  "ds_read_b64 v[106:107], %[va] offset:3240\n" \
  "v_mfma_f32_16x16x32_f16 %[c30], v[112:115], v[68:71], %[c30]\n" \
  "s_waitcnt lgkmcnt(14)\n" \
  "v_mfma_f32_16x16x32_f16 %[c21], v[116:119], v[64:67], %[c21]\n" \
  "ds_read_b64 v[108:109], %[va] offset:3200\n" \
  "s_waitcnt lgkmcnt(14)\n" \
  "ds_read_b64 v[110:111], %[va] offset:3208\n" \
  "v_mfma_f32_16x16x32_f16 %[c31], v[116:119], v[68:71], %[c31]\n" \
  "s_waitcnt lgkmcnt(14)\n" \
  "v_mfma_f32_16x16x32_f16 %[c30], v[96:99], v[64:67], %[c30]\n" \
  "ds_read_b64 v[112:113], %[va] offset:2208\n" \
  "s_waitcnt lgkmcnt(14)\n" \
  "ds_read_b64 v[114:115], %[va] offset:2216\n" \
  "s_waitcnt lgkmcnt(10)\n" \
  "v_mfma_f32_16x16x32_f16 %[c31], v[100:103], v[64:67], %[c31]\n" \
  "ds_read_b64 v[116:117], %[va] offset:2176\n" \
  "ds_read_b64 v[118:119], %[va] offset:2184\n" \
  "s_waitcnt lgkmcnt(6)\n" \
  "v_mfma_f32_16x16x32_f16 %[c00], v[104:107], v[80:83], %[c00]\n" \
  "ds_read_b64 v[96:97], %[va] offset:1184\n" \
  "ds_read_b64 v[98:99], %[va] offset:1192\n" \
  "v_mfma_f32_16x16x32_f16 %[c10], v[104:107], v[84:87], %[c10]\n" \
  "v_mfma_f32_16x16x32_f16 %[c20], v[104:107], v[88:91], %[c20]\n" \
  "v_mfma_f32_16x16x32_f16 %[c30], v[104:107], v[92:95], %[c30]\n" \
  "s_waitcnt lgkmcnt(6)\n" \
  "v_mfma_f32_16x16x32_f16 %[c01], v[108:111], v[80:83], %[c01]\n" \
  "ds_read_b64 v[100:101], %[va] offset:1152\n" \
  "ds_read_b64 v[102:103], %[va] offset:1160\n" \
  "v_mfma_f32_16x16x32_f16 %[c11], v[108:111], v[84:87], %[c11]\n" \
  "v_mfma_f32_16x16x32_f16 %[c21], v[108:111], v[88:91], %[c21]\n" \
  "v_mfma_f32_16x16x32_f16 %[c31], v[108:111], v[92:95], %[c31]\n" \
  "s_waitcnt lgkmcnt(6)\n" \
  "v_mfma_f32_16x16x32_f16 %[c10], v[112:115], v[80:83], %[c10]\n" \
  "ds_read_b64 v[104:105], %[va] offset:160\n" \
  "ds_read_b64 v[106:107], %[va] offset:168\n" \
  "v_mfma_f32_16x16x32_f16 %[c20], v[112:115], v[84:87], %[c20]\n" \
  "ds_read_b64 v[64:65], %[vb] offset:80\n" \
  "ds_read_b64 v[66:67], %[vb] offset:112\n" \
  "v_mfma_f32_16x16x32_f16 %[c30], v[112:115], v[88:91], %[c30]\n" \
  "ds_read_b64 v[68:69], %[vb] offset:1360\n" \
  "ds_read_b64 v[70:71], %[vb] offset:1392\n" \
  "s_waitcnt lgkmcnt(10)\n" \
  "v_mfma_f32_16x16x32_f16 %[c11], v[116:119], v[80:83], %[c11]\n" \
  "ds_read_b64 v[108:109], %[va] offset:128\n" \
  "ds_read_b64 v[110:111], %[va] offset:136\n" \
  "v_mfma_f32_16x16x32_f16 %[c21], v[116:119], v[84:87], %[c21]\n" \
  "ds_read_b64 v[72:73], %[vb] offset:2640\n" \
  "ds_read_b64 v[74:75], %[vb] offset:2672\n" \
  "v_mfma_f32_16x16x32_f16 %[c31], v[116:119], v[88:91], %[c31]\n" \
  "ds_read_b64 v[76:77], %[vb] offset:3920\n" \
  "s_waitcnt lgkmcnt(14)\n" \
  "ds_read_b64 v[78:79], %[vb] offset:3952\n" \
  "s_waitcnt lgkmcnt(14)\n" \
  "v_mfma_f32_16x16x32_f16 %[c20], v[96:99], v[80:83], %[c20]\n" \
  "ds_read_b64 v[112:113], %[va] offset:3168\n" \
  "s_waitcnt lgkmcnt(14)\n" \
  "ds_read_b64 v[114:115], %[va] offset:3176\n" \
  "v_mfma_f32_16x16x32_f16 %[c30], v[96:99], v[84:87], %[c30]\n" \
  "s_waitcnt lgkmcnt(14)\n" \
  "v_mfma_f32_16x16x32_f16 %[c21], v[100:103], v[80:83], %[c21]\n" \
  "ds_read_b64 v[116:117], %[va] offset:3136\n" \
  "s_waitcnt lgkmcnt(14)\n" \
  "ds_read_b64 v[118:119], %[va] offset:3144\n" \
  "v_mfma_f32_16x16x32_f16 %[c31], v[100:103], v[84:87], %[c31]\n" \
  "s_waitcnt lgkmcnt(14)\n" \
  "v_mfma_f32_16x16x32_f16 %[c30], v[104:107], v[80:83], %[c30]\n" \
  "ds_read_b64 v[96:97], %[va] offset:2144\n" \
  "s_waitcnt lgkmcnt(14)\n" \
  "ds_read_b64 v[98:99], %[va] offset:2152\n" \
  "s_waitcnt lgkmcnt(10)\n" \
  "v_mfma_f32_16x16x32_f16 %[c31], v[108:111], v[80:83], %[c31]\n" \
  "ds_read_b64 v[100:101], %[va] offset:2112\n" \
  "ds_read_b64 v[102:103], %[va] offset:2120\n" \
  "s_waitcnt lgkmcnt(6)\n" \
  "v_mfma_f32_16x16x32_f16 %[c00], v[112:115], v[64:67], %[c00]\n" \
  "ds_read_b64 v[104:105], %[va] offset:1120\n" \
  "ds_read_b64 v[106:107], %[va] offset:1128\n" \
  "v_mfma_f32_16x16x32_f16 %[c10], v[112:115], v[68:71], %[c10]\n" \
  "v_mfma_f32_16x16x32_f16 %[c20], v[112:115], v[72:75], %[c20]\n" \
  "v_mfma_f32_16x16x32_f16 %[c30], v[112:115], v[76:79], %[c30]\n" \
  "s_waitcnt lgkmcnt(6)\n" \
  "v_mfma_f32_16x16x32_f16 %[c01], v[116:119], v[64:67], %[c01]\n" \
  "ds_read_b64 v[108:109], %[va] offset:1088\n" \
  "ds_read_b64 v[110:111], %[va] offset:1096\n" \
  "v_mfma_f32_16x16x32_f16 %[c11], v[116:119], v[68:71], %[c11]\n" \
  "v_mfma_f32_16x16x32_f16 %[c21], v[116:119], v[72:75], %[c21]\n" \
  "v_mfma_f32_16x16x32_f16 %[c31], v[116:119], v[76:79], %[c31]\n" \
  "s_waitcnt lgkmcnt(6)\n" \
  "v_mfma_f32_16x16x32_f16 %[c10], v[96:99], v[64:67], %[c10]\n" \
  "ds_read_b64 v[112:113], %[va] offset:96\n" \
  "ds_read_b64 v[114:115], %[va] offset:104\n" \
  "v_mfma_f32_16x16x32_f16 %[c20], v[96:99], v[68:71], %[c20]\n" \
  "ds_read_b64 v[80:81], %[vb] offset:0\n" \
  "ds_read_b64 v[82:83], %[vb] offset:32\n" \
  "v_mfma_f32_16x16x32_f16 %[c30], v[96:99], v[72:75], %[c30]\n" \
  "ds_read_b64 v[84:85], %[vb] offset:1280\n" \
  "ds_read_b64 v[86:87], %[vb] offset:1312\n" \
  "s_waitcnt lgkmcnt(10)\n" \
  "v_mfma_f32_16x16x32_f16 %[c11], v[100:103], v[64:67], %[c11]\n" \
  "ds_read_b64 v[116:117], %[va] offset:64\n" \
  "ds_read_b64 v[118:119], %[va] offset:72\n" \
  "v_mfma_f32_16x16x32_f16 %[c21], v[100:103], v[68:71], %[c21]\n" \
  "ds_read_b64 v[88:89], %[vb] offset:2560\n" \
  "ds_read_b64 v[90:91], %[vb] offset:2592\n" \
  "v_mfma_f32_16x16x32_f16 %[c31], v[100:103], v[72:75], %[c31]\n" \
  "ds_read_b64 v[92:93], %[vb] offset:3840\n" \
  "s_waitcnt lgkmcnt(14)\n" \
  "ds_read_b64 v[94:95], %[vb] offset:3872\n" \
  "s_waitcnt lgkmcnt(14)\n" \
  "v_mfma_f32_16x16x32_f16 %[c20], v[104:107], v[64:67], %[c20]\n" \
  "ds_read_b64 v[96:97], %[va] offset:3104\n" \
  "s_waitcnt lgkmcnt(14)\n" \
  "ds_read_b64 v[98:99], %[va] offset:3112\n" \
  "v_mfma_f32_16x16x32_f16 %[c30], v[104:107], v[68:71], %[c30]\n" \
  "s_waitcnt lgkmcnt(14)\n" \
  "v_mfma_f32_16x16x32_f16 %[c21], v[108:111], v[64:67], %[c21]\n" \
  "ds_read_b64 v[100:101], %[va] offset:3072\n" \
  "s_waitcnt lgkmcnt(14)\n" \
  "ds_read_b64 v[102:103], %[va] offset:3080\n" \
  "v_mfma_f32_16x16x32_f16 %[c31], v[108:111], v[68:71], %[c31]\n" \
  "s_waitcnt lgkmcnt(14)\n" \
  "v_mfma_f32_16x16x32_f16 %[c30], v[112:115], v[64:67], %[c30]\n" \
  "ds_read_b64 v[104:105], %[va] offset:2080\n" \
  "s_waitcnt lgkmcnt(14)\n" \
  "ds_read_b64 v[106:107], %[va] offset:2088\n" \
  "s_waitcnt lgkmcnt(10)\n" \
  "v_mfma_f32_16x16x32_f16 %[c31], v[116:119], v[64:67], %[c31]\n" \
  "ds_read_b64 v[108:109], %[va] offset:2048\n" \
  "ds_read_b64 v[110:111], %[va] offset:2056\n" \
  "s_waitcnt lgkmcnt(6)\n" \
  "v_mfma_f32_16x16x32_f16 %[c00], v[96:99], v[80:83], %[c00]\n" \
  "ds_read_b64 v[112:113], %[va] offset:1056\n" \
  "ds_read_b64 v[114:115], %[va] offset:1064\n" \
  "v_mfma_f32_16x16x32_f16 %[c10], v[96:99], v[84:87], %[c10]\n" \
  "v_mfma_f32_16x16x32_f16 %[c20], v[96:99], v[88:91], %[c20]\n" \
  "v_mfma_f32_16x16x32_f16 %[c30], v[96:99], v[92:95], %[c30]\n" \
  "s_waitcnt lgkmcnt(6)\n" \
  "v_mfma_f32_16x16x32_f16 %[c01], v[100:103], v[80:83], %[c01]\n" \
  "ds_read_b64 v[116:117], %[va] offset:1024\n" \
  "ds_read_b64 v[118:119], %[va] offset:1032\n" \
  "v_mfma_f32_16x16x32_f16 %[c11], v[100:103], v[84:87], %[c11]\n" \
  "v_mfma_f32_16x16x32_f16 %[c21], v[100:103], v[88:91], %[c21]\n" \
  "v_mfma_f32_16x16x32_f16 %[c31], v[100:103], v[92:95], %[c31]\n" \
  "s_waitcnt lgkmcnt(6)\n" \
  "v_mfma_f32_16x16x32_f16 %[c10], v[104:107], v[80:83], %[c10]\n" \
  "ds_read_b64 v[96:97], %[va] offset:32\n" \
  "ds_read_b64 v[98:99], %[va] offset:40\n" \
  "v_mfma_f32_16x16x32_f16 %[c20], v[104:107], v[84:87], %[c20]\n" \
  "v_mfma_f32_16x16x32_f16 %[c30], v[104:107], v[88:91], %[c30]\n" \
  "s_waitcnt lgkmcnt(6)\n" \
  "v_mfma_f32_16x16x32_f16 %[c11], v[108:111], v[80:83], %[c11]\n" \
  "ds_read_b64 v[100:101], %[va] offset:0\n" \
  "ds_read_b64 v[102:103], %[va] offset:8\n" \
  "v_mfma_f32_16x16x32_f16 %[c21], v[108:111], v[84:87], %[c21]\n" \
  "v_mfma_f32_16x16x32_f16 %[c31], v[108:111], v[88:91], %[c31]\n" \
  "s_waitcnt lgkmcnt(6)\n" \
  "v_mfma_f32_16x16x32_f16 %[c20], v[112:115], v[80:83], %[c20]\n" \
  "v_mfma_f32_16x16x32_f16 %[c30], v[112:115], v[84:87], %[c30]\n" \
  "s_waitcnt lgkmcnt(4)\n" \
  "v_mfma_f32_16x16x32_f16 %[c21], v[116:119], v[80:83], %[c21]\n" \
  "v_mfma_f32_16x16x32_f16 %[c31], v[116:119], v[84:87], %[c31]\n" \
  "s_waitcnt lgkmcnt(2)\n" \
  "v_mfma_f32_16x16x32_f16 %[c30], v[96:99], v[80:83], %[c30]\n" \
  "s_waitcnt lgkmcnt(0)\n" \
  "v_mfma_f32_16x16x32_f16 %[c31], v[100:103], v[80:83], %[c31]\n" \
  "s_nop 7\n" \
  ""
#define K2_CLOB "v64","v65","v66","v67","v68","v69","v70","v71","v72","v73","v74","v75","v76","v77","v78","v79","v80","v81","v82","v83","v84","v85","v86","v87","v88","v89","v90","v91","v92","v93","v94","v95","v96","v97","v98","v99","v100","v101","v102","v103","v104","v105","v106","v107","v108","v109","v110","v111","v112","v113","v114","v115","v116","v117","v118","v119","memory"
typedef short s16x4 __attribute__((ext_vector_type(4)));
typedef _Float16 f16x2 __attribute__((ext_vector_type(2)));
typedef _Float16 f16x8 __attribute__((ext_vector_type(8)));
typedef __bf16 bf16x8 __attribute__((ext_vector_type(8)));
typedef __bf16 bf16x2 __attribute__((ext_vector_type(2)));
typedef float f32x2 __attribute__((ext_vector_type(2)));
typedef __amdgpu_buffer_rsrc_t rsrc_t;
#define STAMP_DECL
#define STP(i)
#define STP_ARG
#define STP_PASS
#define STP_OUT(base)
#define LDSP(T, p) ((__attribute__((address_space(3))) T*)(p))
#define GLBP(p) ((const __attribute__((address_space(1))) void*)(p))
#define ST_AUX 16

constexpr int KTB = 1344;
constexpr int K1_LN = 64, K1_KG = 576, K1_RT = 216;
constexpr int K1_LDS = 32 * 1184;

__device__ __forceinline__ rsrc_t ws_rsrc(char* ws) { return __builtin_amdgcn_make_buffer_rsrc((void*)ws, 0, (int)WS_END, 0x00020000); }
__device__ __forceinline__ void st16(rsrc_t r, u32 off, u32x4 v) { __builtin_amdgcn_raw_buffer_store_b128(v, r, (int)off, 0, ST_AUX); }
__device__ __forceinline__ void st8(rsrc_t r, u32 off, u32x2 v) { __builtin_amdgcn_raw_buffer_store_b64(v, r, (int)off, 0, ST_AUX); }
__device__ __forceinline__ void st4(rsrc_t r, u32 off, u32 v) { __builtin_amdgcn_raw_buffer_store_b32(v, r, (int)off, 0, ST_AUX); }

__device__ __forceinline__ u32x2 tr_read(const char* p) {
  s16x4 v = __builtin_amdgcn_ds_read_tr16_b64_v4i16(LDSP(s16x4, p));
  return __builtin_bit_cast(u32x2, v);
}
__device__ __forceinline__ u32 pk_f16(float a, float b) {
  f32x2 v = {a, b};
  return __builtin_bit_cast(u32, __builtin_convertvector(v, f16x2));
}
__device__ __forceinline__ u32 pk_f16v(f32x2 v) { return __builtin_bit_cast(u32, __builtin_convertvector(v, f16x2)); }
__device__ __forceinline__ u32 pk_bf16(float a, float b) {
  f32x2 v = {a, b};
  return __builtin_bit_cast(u32, __builtin_convertvector(v, bf16x2));
}
template <int CTRL>
__device__ __forceinline__ float dpp_mov(float x) {
  return __builtin_bit_cast(float, __builtin_amdgcn_update_dpp(0, __builtin_bit_cast(int, x), CTRL, 0xf, 0xf, true));
}
__device__ __forceinline__ float sum8(float s) {
  s += dpp_mov<0xB1>(s); s += dpp_mov<0x4E>(s); s += dpp_mov<0x141>(s);
  return s;
}

#define CODE_ADDR(var, sym) \
  unsigned long long var; \
  asm volatile("s_getpc_b64 s[90:91]\n\ts_add_u32 s90, s90, " sym "@rel32@lo+4\n\ts_addc_u32 s91, s91, " sym "@rel32@hi+12\n\ts_mov_b64 %0, s[90:91]" : "=s"(var) :: "s90", "s91", "scc");
template <int BYTES, int N> __device__ __forceinline__ void code_touch(unsigned long long base, int lane, u32 (&t)[N]) {
#pragma unroll
  for (int i = 0; i < N; ++i) { const int o = min((i * 64 + lane) * 128, BYTES - 4); t[i] = *(const __attribute__((address_space(1))) u32*)(base + o); }
}
#define CP2_BYTES 6400
constexpr int CP3_N = (CP3_BYTES + 8191) / 8192;

__device__ __forceinline__ void ln_role(char* lds, int wg, const u16* __restrict__ u, rsrc_t rs_ STP_ARG) {
  const int tid = threadIdx.x, lane = tid & 63, v = tid >> 6;
  const int rr = lane >> 3, sub = lane & 7;
  const int ll = 8 * v + rr;
  const u32x4* src = (const u32x4*)(u + (size_t)(32 * wg + ll) * D_) + sub;
  u32x4 x[9];
#pragma unroll
  for (int k = 0; k < 9; ++k) x[k] = src[8 * k];
  const int rho = (((ll >> 2) & 1) << 4) | ((ll >> 3) << 2) | (ll & 3);
  char* wp = lds + rho * 1184 + sub * 16;
#pragma unroll
  for (int k = 0; k < 9; ++k) *LDSP(u32x4, wp + k * 128) = x[k];
  const int gam = lane >> 4, tq = (lane & 15) >> 2, tp = lane & 3;
  const char* rp = lds + (4 * gam + tq) * 1184 + tp * 8;
  const u32 go = (u32)WS_XT + (lane >> 2) * 4096 + wg * 64 + (lane & 3) * 16 + v * 9 * 65536;
  const int bsrc = (16 * (lane & 3) + (lane >> 2)) * 4;
  __syncthreads();
  STP(1)
#pragma unroll
  for (int k = 0; k < 9; ++k) {
    const char* q = rp + (9 * v + k) * 32;
    u32x2 a = tr_read(q), b = tr_read(q + 16 * 1184);
    const u32 a0 = a[0], a1 = a[1], b0 = b[0], b1 = b[1];
    u32x4 o = {(u32)__builtin_amdgcn_ds_bpermute(bsrc, (int)a0), (u32)__builtin_amdgcn_ds_bpermute(bsrc, (int)a1),
               (u32)__builtin_amdgcn_ds_bpermute(bsrc, (int)b0), (u32)__builtin_amdgcn_ds_bpermute(bsrc, (int)b1)};
    st16(rs_, go + k * 65536, o);
  }
  STP(2)
  f32x2 f[36];
  f32x2 s2 = {0.f, 0.f};
#pragma unroll
  for (int k = 0; k < 9; ++k)
#pragma unroll
    for (int i = 0; i < 4; ++i) { const u32 w = x[k][i]; f[4 * k + i] = f32x2{blo(w), bhi(w)}; s2 += f[4 * k + i]; }
  const float m = sum8(s2[0] + s2[1]) * (1.0f / D_);
  const f32x2 m2 = {m, m};
  f32x2 q2 = {0.f, 0.f};
#pragma unroll
  for (int k = 0; k < 36; ++k) { const f32x2 a = f[k] - m2; q2 = __builtin_elementwise_fma(a, a, q2); }
  const float q = sum8(q2[0] + q2[1]);
  const float mb = rbf(m);
  const float sd = rbf(sqrtf(rbf(rbf(q * (1.0f / D_)) + 1e-5f)));
  STP(3)
  if (sub == 0) {
    const int l = 32 * wg + ll;
    st4(rs_, (u32)WS_ST + ((l >> 2) & 1) * 4096 + (l >> 3) * 16 + (l & 3) * 4, (__float_as_uint(mb) >> 16) | (__float_as_uint(sd) & 0xffff0000u));
  }
}

__device__ __forceinline__ f32x2 cmul(f32x2 a, f32x2 b, f32x2 bs) {
  return __builtin_elementwise_fma(a.yy, bs, a.xx * b);
}
__device__ __forceinline__ f32x2 csw(f32x2 b) { return f32x2{-b.y, b.x}; }
__device__ __forceinline__ f32x2 cexp_p(float zr, float rh, float rl, float p) {
  const float e = __builtin_amdgcn_exp2f(zr * p * 1.44269504088896341f);
  const float t = __builtin_amdgcn_fractf(rh * p) + rl * p;
  return f32x2{e * __builtin_amdgcn_cosf(t), e * __builtin_amdgcn_sinf(t)};
}
__device__ __forceinline__ float fexp(float x) { return __builtin_amdgcn_exp2f(x * 1.44269504088896341f); }
__device__ __forceinline__ u32 dup16(u32 a) { return a | (a << 16); }

__device__ __forceinline__ void kgen_role(char* lds, int d, const u16* __restrict__ logA, const u16* __restrict__ Aim, const u16* __restrict__ Cr,
                                          const u16* __restrict__ Ci, const u16* __restrict__ logdt, rsrc_t rs_ STP_ARG) {
  constexpr int RS = 528;
  constexpr int GOFF = 0, EOFF = 21 * RS, TOFF = EOFF + 32 * RS;
  const int tid = threadIdx.x, lane = tid & 63, v = __builtin_amdgcn_readfirstlane(tid >> 6);
  const int n = tid & 127;
  const u16 la_ = logA[d * N_ + n], ai_ = Aim[d * N_ + n], ldt_ = logdt[d];
  const u16 cr_ = Cr[d * N_ + n], ci_ = Ci[d * N_ + n], cr2_ = Cr[d * N_ + (n ^ 64)], ci2_ = Ci[d * N_ + (n ^ 64)];
  asm volatile("" :: "v"((u32)la_), "v"((u32)ai_), "v"((u32)ldt_), "v"((u32)cr_), "v"((u32)ci_), "v"((u32)cr2_), "v"((u32)ci2_));
  const float la = bf2f(la_), ai = bf2f(ai_);
  const float dt = rbf(fexp(bf2f(ldt_)));
  STP(1)
  if (v < 2) {
    const float zr = -rbf(fexp(la)) * dt, zi = ai * dt;
    const float rh = zi * 0.15915493667125702f;
    const float rl = __builtin_fmaf(zi, 0.15915493667125702f, -rh) + zi * 6.420638326565253e-09f;
    const f32x2 w = cexp_p(zr, rh, rl, 8.f);
    const f32x2 w2 = cmul(w, w, csw(w)), w2s = csw(w2);
    f32x2 a = {1.f, 0.f}, b = w;
    char* ep = lds + EOFF + n * 4;
#pragma unroll
    for (int j = 0; j < 32; j += 2) {
      *LDSP(u32, ep + j * RS) = pk_f16v(a);
      *LDSP(u32, ep + (j + 1) * RS) = pk_f16v(b);
      if (j < 30) { a = cmul(a, w2, w2s); b = cmul(b, w2, w2s); }
    }
  } else {
    const float cr = bf2f(cr_) * dt, ci = bf2f(ci_) * dt;
    const float cr2 = bf2f(cr2_) * dt, ci2 = bf2f(ci2_) * dt;
    const float zr = -rbf(fexp(la)) * dt, zi = -ai * dt;
    const float rh = zi * 0.15915493667125702f;
    const float rl = __builtin_fmaf(zi, 0.15915493667125702f, -rh) + zi * 6.420638326565253e-09f;
    float mx = fmaxf(fmaxf(fabsf(cr), fabsf(ci)), fmaxf(fabsf(cr2), fabsf(ci2)));
    mx = fmaxf(mx, dpp_mov<0x128>(mx)); mx = fmaxf(mx, dpp_mov<0x124>(mx)); mx = fmaxf(mx, dpp_mov<0x122>(mx)); mx = fmaxf(mx, dpp_mov<0x121>(mx));
    const int mi = __builtin_bit_cast(int, mx);
    const u32 m01 = max((u32)__builtin_amdgcn_readlane(mi, 0), (u32)__builtin_amdgcn_readlane(mi, 16));
    const u32 m23 = max((u32)__builtin_amdgcn_readlane(mi, 32), (u32)__builtin_amdgcn_readlane(mi, 48));
    mx = fmaxf(__uint_as_float(max(m01, m23)), 1e-30f);
    const float inv = __builtin_amdgcn_rcpf(mx);
    if (tid == 128) st4(rs_, (u32)WS_KS + d * 4, __float_as_uint(2.0f * mx));
    const f32x2 v1 = cexp_p(zr, rh, rl, 1.f), t256 = cexp_p(zr, rh, rl, 256.f);
    const f32x2 v1s = csw(v1), t256s = csw(t256);
    const f32x2 v2 = cmul(v1, v1, v1s), v2s = csw(v2), v4 = cmul(v2, v2, v2s), v4s = csw(v4);
    const f32x2 t512 = cmul(t256, t256, t256s), t512s = csw(t512), t1024 = cmul(t512, t512, t512s);
    const f32x2 c = {cr * inv, -ci * inv};
    char* gp = lds + GOFF + n * 4;
    auto put = [&](int row, f32x2 g) { *LDSP(u32, gp + row * RS) = pk_f16v(g); };
    f32x2 g = c;
#pragma unroll
    for (int a = 0; a < 8; ++a) { put(a, g); if (a < 7) g = cmul(g, v1, v1s); }
    g = cmul(c, t256, t256s); put(8, g);
    g = cmul(g, v2, v2s); put(9, g); g = cmul(g, v2, v2s); put(10, g); g = cmul(g, v2, v2s); put(11, g);
    g = cmul(c, t512, t512s); put(12, g); put(13, cmul(g, v4, v4s));
    g = cmul(g, t256, t256s); put(14, g); put(15, cmul(g, v4, v4s));
    g = cmul(c, t1024, csw(t1024)); put(16, g);
    g = cmul(g, t256, t256s); put(17, g); g = cmul(g, t256, t256s); put(18, g); g = cmul(g, t256, t256s); put(19, g);
    g = cmul(g, t256, t256s); put(20, g);
  }
  __syncthreads();
  STP(2)
  const int r = lane & 15, g4 = lane >> 4, mt = v >> 1, nt = v & 1;
  const char* ap = lds + GOFF + (16 * mt + r) * RS + g4 * 16;
  const char* bp = lds + EOFF + (16 * nt + r) * RS + g4 * 16;
  f32x4 acc = {0.f, 0.f, 0.f, 0.f};
#pragma unroll
  for (int kk = 0; kk < 8; ++kk) {
    f16x8 a = *LDSP(const f16x8, ap + kk * 64), b = *LDSP(const f16x8, bp + kk * 64);
    acc = __builtin_amdgcn_mfma_f32_16x16x32_f16(a, b, acc, 0, 0, 0);
  }
  char* T = lds + TOFF;
  const int j = 16 * nt + r;
  if (mt == 0) {
    const int ia = g4 < 2 ? 4 * g4 + 8 * j : g4 == 2 ? 256 + 4 * j : 384 + 2 * j;
    const int ib = ia + (g4 == 3 ? 64 : 2);
    *LDSP(u32, T + ia * 2) = pk_f16(acc[0], acc[1]);
    *LDSP(u32, T + ib * 2) = pk_f16(acc[2], acc[3]);
  } else if (g4 < 2) {
    const u32 p0 = pk_f16(acc[0], acc[1]), p1 = pk_f16(acc[2], acc[3]);
    char* q = T + (512 + 128 * g4 + j) * 2;
    *LDSP(u16, q) = (u16)p0; *LDSP(u16, q + 64) = (u16)(p0 >> 16); *LDSP(u16, q + 128) = (u16)p1; *LDSP(u16, q + 192) = (u16)(p1 >> 16);
  }
  __syncthreads();
  STP(3)
  if (tid < KTB / 16) st16(rs_, (u32)WS_K + d * KTB + tid * 16, *LDSP(const u32x4, T + tid * 16));
  STP(4)
}

__device__ __forceinline__ void rt1_role(char* lds, int wg, const u16* __restrict__ w1, rsrc_t rs_ STP_ARG) {
  const int tid = threadIdx.x, lane = tid & 63, v = __builtin_amdgcn_readfirstlane(tid >> 6);
  const int T = wg / 3, kg = wg - 3 * T;
  __builtin_amdgcn_s_sleep(RT1_DELAY);
  {
    const int rr = lane >> 3, c16 = lane & 7;
#pragma unroll
    for (int k = 0; k < 6; ++k) {
      const int row = 48 * v + 8 * k + rr;
      const int f = ((row >> 1) & 1) | (((row >> 3) & 1) << 1);
      const u16* g = w1 + (size_t)(192 * kg + row) * F_ + 64 * T + (((c16 >> 1) ^ f) * 16) + (c16 & 1) * 8;
      __builtin_amdgcn_global_load_lds(GLBP(g), LDSP(void, lds + (48 * v + 8 * k) * 128), 16, 0, 0);
    }
  }
  asm volatile("s_waitcnt vmcnt(0)" ::: "memory");
  __syncthreads();
  STP(1)
  const int g4 = lane >> 4, tq = (lane & 15) >> 2, tp = lane & 3;
  const int fsw = (tq >> 1) | ((g4 & 1) << 1);
  const char* rp = lds + (8 * g4 + tq) * 128 + ((v ^ fsw) * 32) + tp * 8;
  const int B = 4 * T + v, s = B / 36, w = (B % 36) / 9, b = B % 9;
  const u32 dst = (u32)WS_WT + (u32)(s * 4 + w) * (u32)WT_STREAM + (u32)(18 * b + 6 * kg) * 1024 + lane * 16;
  f32x4 acc = {0.f, 0.f, 0.f, 0.f};
  const u32x4 onesu = {0x3f803f80u, 0x3f803f80u, 0x3f803f80u, 0x3f803f80u};
#pragma unroll
  for (int k = 0; k < 6; ++k) {
    u32x2 a = tr_read(rp + k * 4096), c = tr_read(rp + k * 4096 + 512);
    u32x4 o = {a[0], a[1], c[0], c[1]};
    st16(rs_, dst + k * 1024, o);
    acc = __builtin_amdgcn_mfma_f32_16x16x32_bf16(__builtin_bit_cast(bf16x8, o), __builtin_bit_cast(bf16x8, onesu), acc, 0, 0, 0);
  }
  STP(4)
  if ((lane & 15) == 0) st16(rs_, (u32)WS_CS + (kg * F_ + 16 * B + 4 * g4) * 4, __builtin_bit_cast(u32x4, acc));
}

#define K2_NCB 0
constexpr int K2_CV = 576, K2_RT = 216, K2_CB = K2_NCB;
constexpr int K2_KC = 0, K2_UI = 16640, K2_TB = 23040, K2_LDS = 36864;

__device__ __forceinline__ void conv_role(char* lds, int d, const char* __restrict__ ws, const u16* __restrict__ Dsk, rsrc_t rs_ STP_ARG) {
  const int tid = threadIdx.x, lane = tid & 63, v = __builtin_amdgcn_readfirstlane(tid >> 6);
  const int r = lane & 15, g = lane >> 4;
  const char* xr = ws + WS_XT + (size_t)d * 4096;
  const u32x4 xin = *(const u32x4*)(xr + tid * 16);
  const u32x4 st0 = *(const u32x4*)(ws + WS_ST + tid * 16), st1 = *(const u32x4*)(ws + WS_ST + 4096 + tid * 16);
  if (tid < KTB / 16)
    __builtin_amdgcn_global_load_lds(GLBP(ws + WS_K + (size_t)d * KTB + tid * 16), LDSP(void, lds + K2_TB + v * 1024), 16, 0, 0);
  const float sc = *(const float*)(ws + WS_KS + d * 4), dsk = bf2f(Dsk[d]);
  if (tid < 80) { u32x4 z = {0, 0, 0, 0}; *LDSP(u32x4, lds + K2_UI + tid * 16) = z; }
  u32 lnb[4];
  {
    u32 o[4];
#pragma unroll
    for (int k = 0; k < 4; ++k) {
      const u32 xw = xin[k];
      const u32 sa = k < 2 ? st0[2 * k] : st1[2 * k - 4], sb = k < 2 ? st0[2 * k + 1] : st1[2 * k - 3];
      const u32 c = pk_bf16(blo(xw) - blo(sa), bhi(xw) - blo(sb));
      lnb[k] = pk_bf16(blo(c) * __builtin_amdgcn_rcpf(bhi(sa)), bhi(c) * __builtin_amdgcn_rcpf(bhi(sb)));
      o[k] = pk_f16(blo(lnb[k]), bhi(lnb[k]));
    }
    char* wp = lds + K2_UI + (16 + (tid >> 2)) * 80 + (tid & 3) * 8;
    *LDSP(u32x2, wp) = u32x2{o[0], o[1]};
    *LDSP(u32x2, wp + 32) = u32x2{o[2], o[3]};
  }
  STP(1)
  asm volatile("s_waitcnt vmcnt(0)" ::: "memory");
  __syncthreads();
  {
    const char* T = lds + K2_TB;
    u32x4 o;
    if (v < 2) {
      const u32 A = *LDSP(const u16, T + (639 - tid) * 2), B = *LDSP(const u16, T + (640 - tid) * 2);
      const u32 tie = (tid & 1) ? A : B;
      o = u32x4{dup16(B), B | (tie << 16), dup16(A), dup16(A)};
    } else if (v == 2) {
      const char* q = T + (766 - 2 * tid) * 2;
      const u32 AB = *LDSP(const u32, q), C = *LDSP(const u16, q + 4);
      const u32 A = AB & 0xffffu, B = AB >> 16;
      o = u32x4{dup16(C), dup16(B), B | (A << 16), dup16(A)};
    } else if (lane < 32) {
      const char* q = T + (1148 - 4 * tid) * 2;
      const u32x2 t = *LDSP(const u32x2, q);
      const u32 E = *LDSP(const u16, q + 8);
      const u32 t0 = t[0], t1 = t[1];
      const u32 A = t0 & 0xffffu, B = t0 >> 16, C = t1 & 0xffffu, Dv = t1 >> 16;
      o = u32x4{E | (Dv << 16), dup16(C), C | (B << 16), dup16(A)};
    } else {
      const u32x4 t = *LDSP(const u32x4, T + (2040 - 8 * tid) * 2);
      const u32 t0 = t[0], t1 = t[1], t2 = t[2], t3 = t[3];
      o = u32x4{__builtin_amdgcn_alignbit(t3, t3, 16), __builtin_amdgcn_alignbit(t2, t2, 16), __builtin_amdgcn_alignbit(t1, t1, 16),
                __builtin_amdgcn_alignbit(t0, t0, 16)};
    }
    char* kc = lds + K2_KC + tid * 16;
    *LDSP(u32x4, kc) = o;
    if (tid < 16) { u32x4 z = {0, 0, 0, 0}; *LDSP(u32x4, lds + K2_KC + (tid >> 2) * 4160 + 4096 + (tid & 3) * 16) = z; }
    __syncthreads();
    const u32x2 nx = *LDSP(const u32x2, kc + 16);
    const u32 o0 = o[0], o1 = o[1], o2 = o[2], o3 = o[3], n0 = nx[0], n1 = nx[1];
    const u32 s1 = __builtin_amdgcn_alignbit(o2, o1, 16), s2 = __builtin_amdgcn_alignbit(o3, o2, 16), s3 = __builtin_amdgcn_alignbit(n0, o3, 16);
    *LDSP(u32x4, kc + 4160) = u32x4{__builtin_amdgcn_alignbit(o1, o0, 16), s1, s2, s3};
    *LDSP(u32x4, kc + 2 * 4160) = u32x4{o1, o2, o3, n0};
    *LDSP(u32x4, kc + 3 * 4160) = u32x4{s1, s2, s3, __builtin_amdgcn_alignbit(n1, n0, 16)};
  }
  __syncthreads();
  STP(2)
  const u32 lb = (u32)(uintptr_t)LDSP(char, lds);
  const u32 va = lb + K2_KC + (((3 - r) & 3) * 520 + 511 - (r >> 2) + 2 * g - 32 * v - 412) * 8;
  const u32 vb = lb + K2_UI + (13 + r - 4 * v) * 80 + 8 * g;
  f32x4 c00, c01, c10, c11, c20, c21, c30, c31;
  asm volatile(K2_ASM
               : [c00] "=&v"(c00), [c01] "=&v"(c01), [c10] "=&v"(c10), [c11] "=&v"(c11), [c20] "=&v"(c20), [c21] "=&v"(c21), [c30] "=&v"(c30),
                 [c31] "=&v"(c31)
               : [va] "v"(va), [vb] "v"(vb)
               : K2_CLOB);
  STP(3)
  __syncthreads();
  {
    char* mine = lds + v * 9216 + r * 144 + g * 16;
    *LDSP(f32x4, mine + 0 * 2304) = c00; *LDSP(f32x4, mine + 0 * 2304 + 64) = c01;
    *LDSP(f32x4, mine + 1 * 2304) = c10; *LDSP(f32x4, mine + 1 * 2304 + 64) = c11;
    *LDSP(f32x4, mine + 2 * 2304) = c20; *LDSP(f32x4, mine + 2 * 2304 + 64) = c21;
    *LDSP(f32x4, mine + 3 * 2304) = c30; *LDSP(f32x4, mine + 3 * 2304 + 64) = c31;
  }
  __syncthreads();
  const char* q = lds + (tid >> 2) * 144 + (tid & 3) * 32;
  f32x4 y0 = *LDSP(const f32x4, q), y1 = *LDSP(const f32x4, q + 16);
#pragma unroll
  for (int o = 1; o < 4; ++o) { y0 += *LDSP(const f32x4, q + o * 9216); y1 += *LDSP(const f32x4, q + o * 9216 + 16); }
  u32x4 ho;
#pragma unroll
  for (int k = 0; k < 4; ++k) {
    const float ya = k < 2 ? y0[2 * k] : y1[2 * k - 4], yb2 = k < 2 ? y0[2 * k + 1] : y1[2 * k - 3];
    const u32 xw = xin[k], lw = lnb[k];
    const u32 yb = pk_bf16(ya * sc, yb2 * sc);
    const u32 sk = pk_bf16(blo(lw) * dsk, bhi(lw) * dsk);
    const u32 t = pk_bf16(blo(yb) + blo(sk), bhi(yb) + bhi(sk));
    ho[k] = pk_bf16(blo(xw) + blo(t), bhi(xw) + bhi(t));
  }
  st16(rs_, (u32)WS_HT + d * 4096 + tid * 16, ho);
  STP(4)
}

__device__ __forceinline__ void rt2_role(char* lds, int wg, const u16* __restrict__ w2, rsrc_t rs_ STP_ARG) {
  const int tid = threadIdx.x, lane = tid & 63, v = __builtin_amdgcn_readfirstlane(tid >> 6);
  const int s = wg / 27, rem = wg - 27 * s, T = rem / 3, kg = rem - 3 * T;
  __builtin_amdgcn_s_sleep(RT2_DELAY);
  {
    const int rr = lane >> 3, c16 = lane & 7;
#pragma unroll
    for (int k = 0; k < 6; ++k) {
      const int row = 48 * v + 8 * k + rr;
      const int ks = 6 * kg + (row >> 5), i = row & 31;
      const int fA = ks < 16 ? 144 * (ks >> 2) + 32 * (ks & 3) : 288 * (ks - 16) + 128;
      const int f = ks < 16 ? fA + i : (i < 16 ? fA + i : fA + 144 + (i - 16));
      const int sw = (row >> 1) & 3;
      const u16* gp = w2 + (size_t)(576 * s + f) * D_ + 64 * T + (((c16 >> 1) ^ sw) * 16) + (c16 & 1) * 8;
      __builtin_amdgcn_global_load_lds(GLBP(gp), LDSP(void, lds + (48 * v + 8 * k) * 128), 16, 0, 0);
    }
  }
  asm volatile("s_waitcnt vmcnt(0)" ::: "memory");
  __syncthreads();
  STP(1)
  const int g4 = lane >> 4, tq = (lane & 15) >> 2, tp = lane & 3;
  const int row0 = 4 * g4 + tq;
  const char* rpA = lds + row0 * 128 + ((v ^ ((row0 >> 1) & 3)) * 32) + tp * 8;
  const char* rpB = rpA + 16 * 128;
  const int j = 4 * T + v, w = j / 9, b = j % 9;
  const u32 dst = (u32)WS_WT + (u32)(s * 4 + w) * (u32)WT_STREAM + (u32)(162 + 18 * b + 6 * kg) * 1024 + lane * 16;
#pragma unroll
  for (int k = 0; k < 6; ++k) {
    u32x2 a = tr_read(rpA + k * 4096), c = tr_read(rpB + k * 4096);
    u32x4 o = {a[0], a[1], c[0], c[1]};
    st16(rs_, dst + k * 1024, o);
  }
}

__device__ __forceinline__ void cb_role(int wg, const char* __restrict__ ws, const u16* __restrict__ b1, rsrc_t rs_) {
  const int f = wg * 256 + threadIdx.x;
  const float* csP = (const float*)(ws + WS_CS);
  const int s = f / 576, fl = f - 576 * s;
  st4(rs_, (u32)WS_CB + (s * 1152 + fl) * 4, __float_as_uint(csP[f] + csP[F_ + f] + csP[2 * F_ + f]));
  st4(rs_, (u32)WS_CB + (s * 1152 + 576 + fl) * 4, __float_as_uint(bf2f(b1[f])));
}

#define K1_R1 0
#define K1_R2 0
#define K2_R1 216
#define PRIO 1
#define KG_FIRST 0
#define PF_OFF 0
#define K1_TW 0
#define TW_SH 16
constexpr int K1_N = K1_LN + K1_KG + K1_R1 + K1_R2 + K1_PF + K1_TW, K2_N = K2_CV + K2_R1 + K2_R2 + K2_CB;
extern "C" __global__ __launch_bounds__(256, 4) void k1_front(const u16* __restrict__ u, const u16* __restrict__ logA, const u16* __restrict__ Aim,
                                                   const u16* __restrict__ Cr, const u16* __restrict__ Ci, const u16* __restrict__ logdt,
                                                   const u16* __restrict__ w1, const u16* __restrict__ w2, char* __restrict__ ws
) {
  __shared__ __attribute__((aligned(16))) char lds[K1_LDS];
  const int b = blockIdx.x;
  STAMP_DECL STP(0)
  asm volatile("" :: "s"(u), "s"(logA), "s"(Aim), "s"(Cr), "s"(Ci), "s"(logdt), "s"(w1), "s"(w2), "s"(ws));
  const rsrc_t rs_ = ws_rsrc(ws);
  if (b < K1_LN) __builtin_amdgcn_s_setprio(3);
  else if (b < 256) __builtin_amdgcn_s_setprio(2);
  else if (b < 512) __builtin_amdgcn_s_setprio(1);
  if (b < 3) { u32x4 z = {0, 0, 0, 0}; st16(rs_, (u32)WS_FLG + b * 4096 + threadIdx.x * 16, z); }
  u32 ct2[1] = {0}, ct3[CP3_N] = {};
  if (b >= K1_LN && b < K1_LN + CPF1 && threadIdx.x >= 192) {
    CODE_ADDR(a2, "k2_conv") code_touch<CP2_BYTES>(a2, threadIdx.x & 63, ct2);
    CODE_ADDR(a3, "k3_mlp") code_touch<CP3_BYTES>(a3, threadIdx.x & 63, ct3);
  }
  if (b < K1_LN) ln_role(lds, b, u, rs_ STP_PASS);
  else if (b < K1_LN + K1_KG) kgen_role(lds, b - K1_LN, logA, Aim, Cr, Ci, logdt, rs_ STP_PASS);
  else if (b < K1_LN + K1_KG + K1_R1) rt1_role(lds, b - K1_LN - K1_KG, w1, rs_ STP_PASS);
  else if (b < K1_LN + K1_KG + K1_R1 + K1_R2) rt2_role(lds, b - K1_LN - K1_KG - K1_R1, w2, rs_ STP_PASS);
  else {
    const int pf = b - (K1_LN + K1_KG + K1_R1 + K1_R2) + PF_OFF;
    const u32* q = (const u32*)(pf < 81 ? w2 : w1) + ((size_t)(pf < 81 ? pf : pf - 81) * 1024 + threadIdx.x) * 16;
    u32 a0 = q[0], a1 = q[256 * 16], a2 = q[512 * 16], a3 = q[768 * 16];
    asm volatile("" :: "v"(a0), "v"(a1), "v"(a2), "v"(a3));
  }
  asm volatile("" :: "v"(ct2[0]));
  for (int i = 0; i < CP3_N; ++i) asm volatile("" :: "v"(ct3[i]));
  STP_OUT(12288)
}
extern "C" __global__ __launch_bounds__(256, 4) void k2_conv(const u16* __restrict__ Dsk, const u16* __restrict__ w1, const u16* __restrict__ w2,
                                                  const u16* __restrict__ b1, char* __restrict__ ws) {
  __shared__ __attribute__((aligned(16))) char lds[K2_LDS];
  const int b = blockIdx.x;
  STAMP_DECL STP(0)
  asm volatile("" :: "s"(Dsk), "s"(w1), "s"(w2), "s"(b1), "s"(ws));
  const rsrc_t rs_ = ws_rsrc(ws);
  if (b < 256) __builtin_amdgcn_s_setprio(3);
  else if (b < 512) __builtin_amdgcn_s_setprio(2);
  else if (b < K2_CV) __builtin_amdgcn_s_setprio(1);
  u32 ct3[CP3_N] = {};
  if (b >= CPF2 && b < CPF2 + 16 && threadIdx.x >= 192) { CODE_ADDR(a3, "k3_mlp") code_touch<CP3_BYTES>(a3, threadIdx.x & 63, ct3); }
  if (b < K2_CV) conv_role(lds, b, ws, Dsk, rs_ STP_PASS);
  else if (b < K2_CV + K2_CB) cb_role(b - K2_CV, ws, b1, rs_);
  else if (b < K2_CV + K2_CB + K2_R1) rt1_role(lds, b - K2_CV - K2_CB, w1, rs_ STP_PASS);
  else rt2_role(lds, b - K2_CV - K2_CB - K2_R1, w2, rs_ STP_PASS);
  for (int i = 0; i < CP3_N; ++i) asm volatile("" :: "v"(ct3[i]));
  STP_OUT(24576)
}
typedef __attribute__((address_space(1))) u32 gu32;
#define OUT_AUX -1
#define SENT 0
#define POLL_GAP 8
extern "C" __global__ __launch_bounds__(256, 1) void k3_mlp(char* __restrict__ ws, const u16* __restrict__ w2, const u16* __restrict__ b1,
                                                  const u16* __restrict__ b2, u16* __restrict__ out
) {
  __shared__ __attribute__((aligned(1024))) char lds[K3_LDS];
  const int tid = threadIdx.x;
  const u32 lane = tid & 63;
  const u32 w = __builtin_amdgcn_readfirstlane(tid >> 6);
  const u32 xcd = blockIdx.x & 7, bi = blockIdx.x >> 3;
  const u32 s = K3_G * (xcd & (8 / K3_G - 1)) + (bi & (K3_G - 1));
  const u32 mt = (32 / K3_G) * (xcd / (8 / K3_G)) + bi / K3_G;
  char* Wt = ws + WS_WT;
  const char* hT = ws + WS_HT;
  char* part = ws + WS_PART;
  #define RFL(x) ((u32)__builtin_amdgcn_readfirstlane((int)(x)))
  const u32 wq = s >> 1, tp = s & 1;
  const u32 rdA = RFL(((mt * 36 + 9 * wq + w) * 2 + tp) * 1024);
  __amdgpu_buffer_rsrc_t rsF = __builtin_amdgcn_make_buffer_rsrc((void*)(ws + WS_FLG + mt * 128), 0, 2 * 4096 + 128, 0x00020000);
  __amdgpu_buffer_rsrc_t rsA = __builtin_amdgcn_make_buffer_rsrc((void*)part, 0, 8 * L_ * D_ * 2, 0x00020000);
  __amdgpu_buffer_rsrc_t rsB = __builtin_amdgcn_make_buffer_rsrc((void*)(b1 + s * 576), 0, 576 * 2, 0x00020000);
  __amdgpu_buffer_rsrc_t rsW = __builtin_amdgcn_make_buffer_rsrc((void*)(Wt + (size_t)(s * 4 + w) * WT_STREAM), 0, (int)WT_STREAM + 65536, 0x00020000);
  __amdgpu_buffer_rsrc_t rsH = __builtin_amdgcn_make_buffer_rsrc((void*)(hT + mt * 128), 0, D_ * 4096, 0x00020000);
  __amdgpu_buffer_rsrc_t rsC = __builtin_amdgcn_make_buffer_rsrc((void*)(ws + WS_CS + s * 2304), 0, 2 * F_ * 4 + 2304, 0x00020000);
  __amdgpu_buffer_rsrc_t rsP = __builtin_amdgcn_make_buffer_rsrc((void*)(part + ((size_t)(s * 32 + mt) * 36 + 9 * w) * 2048), 0, 9 * 2048, 0x00020000);
  const u32 ldsb = (u32)(uintptr_t)(__attribute__((address_space(3))) char*)lds;
  u32x4 pa0, pa1, pa2, pa3, pa4, pa5, pa6, pa7;
  u32 pB;
  asm volatile(K3_ASM
               : [pa0] "=&v"(pa0), [pa1] "=&v"(pa1), [pa2] "=&v"(pa2), [pa3] "=&v"(pa3), [pa4] "=&v"(pa4), [pa5] "=&v"(pa5), [pa6] "=&v"(pa6), [pa7] "=&v"(pa7), [pB] "=&v"(pB)
               : [lane] "v"(lane), [w] "s"(w), [mt] "s"(mt), [ldsb] "s"(ldsb), [rsW] "s"(rsW), [rsH] "s"(rsH), [rsC] "s"(rsC), [rsP] "s"(rsP),
                 [rsF] "s"(rsF), [rsB] "s"(rsB), [rsA] "s"(rsA), [rdA] "s"(rdA)
               : K3_CLOB);
  {
    gu32* cB = (gu32*)(ws + WS_FLG + 4096 + mt * 128);
    gu32* cC = (gu32*)(ws + WS_FLG + 8192 + mt * 128);
    const int v = w, r = lane & 15, g = lane >> 4;
    auto wait32 = [&](gu32* c) {
      u32 pa_ = __hip_atomic_load(c, __ATOMIC_RELAXED, __HIP_MEMORY_SCOPE_AGENT);
      __builtin_amdgcn_s_sleep(POLL_GAP);
      u32 pb_ = __hip_atomic_load(c, __ATOMIC_RELAXED, __HIP_MEMORY_SCOPE_AGENT);
      for (int it = 0; it < (1 << 19); ++it) {
        if (RFL(pa_) >= 32u) break;
        pa_ = __hip_atomic_load(c, __ATOMIC_RELAXED, __HIP_MEMORY_SCOPE_AGENT);
        if (RFL(pb_) >= 32u) break;
        pb_ = __hip_atomic_load(c, __ATOMIC_RELAXED, __HIP_MEMORY_SCOPE_AGENT);
      }
    };
#define TT(i)
    char* ol = lds;
    auto sum8 = [&](const u32x4 (&p)[8], float (&a)[8]) {
#pragma unroll
      for (int k = 0; k < 8; ++k) a[k] = 0.f;
#pragma unroll
      for (int q = 0; q < 8; ++q)
#pragma unroll
        for (int k = 0; k < 4; ++k) { a[2 * k] += blo(p[q][k]); a[2 * k + 1] += bhi(p[q][k]); }
    };
    auto finish = [&](const float (&a)[8], int jl, u32x2 bb, const u16 (&h)[8]) {
      const float bv[4] = {blo(bb[0]), bhi(bb[0]), blo(bb[1]), bhi(bb[1])};
#pragma unroll
      for (int u = 0; u < 2; ++u) {
        float o[4];
#pragma unroll
        for (int k = 0; k < 4; ++k) o[k] = bf2f(h[4 * u + k]) + rbf(rbf(a[4 * u + k]) + bv[k]);
        *LDSP(u32x2, ol + (16 * u + r) * 304 + jl * 32 + g * 8) = u32x2{pk_bf16(o[0], o[1]), pk_bf16(o[2], o[3])};
      }
    };
    auto fetch8 = [&](u32x4 (&p)[8], u32 off) {
#pragma unroll
      for (int q = 0; q < 8; ++q) p[q] = __builtin_amdgcn_raw_buffer_load_b128(rsA, (int)(lane * 16), (int)(off + q * (32 * 72 * 1024)), 16);
    };
    const u16* hb = (const u16*)(hT + (size_t)(144 * wq + 4 * g) * 4096 + mt * 128 + tp * 64) + r;
    const u16* bq = b2 + 144 * wq + 4 * g;
    auto fetch_h = [&](u16 (&h)[8], int jl) {
#pragma unroll
      for (int k = 0; k < 8; ++k) h[k] = hb[(size_t)(16 * jl + (k & 3)) * 2048 + 16 * (k >> 2)];
    };
    u16 hA[8], hB[8], hC[8];
    fetch_h(hA, v);
    fetch_h(hB, 4 + v);
    const u32x2 bA = *(const u32x2*)(bq + 16 * v), bB = *(const u32x2*)(bq + 16 * (4 + v)), bC = *(const u32x2*)(bq + 128);
    float aA[8];
    u32x4 pb[8];
    u32 c0 = 0;
    if (v < 3) {
      if (RFL(pB) < 32u) wait32(cB);
      fetch8(pb, rdA + 4 * 2048);
    } else {
      fetch_h(hC, 8);
      c0 = __hip_atomic_load(cC, __ATOMIC_RELAXED, __HIP_MEMORY_SCOPE_AGENT);
    }
    { const u32x4 pa[8] = {pa0, pa1, pa2, pa3, pa4, pa5, pa6, pa7}; sum8(pa, aA); }
    TT(1)
    finish(aA, v, bA, hA);
    TT(2)
    if (v < 3) { float a[8]; sum8(pb, a); finish(a, 4 + v, bB, hB); }
    else {
      if (RFL(c0) < 32u) wait32(cC);
      TT(0)
      u32x4 pc[8];
      fetch8(pb, rdA + 4 * 2048);
      fetch8(pc, rdA + 5 * 2048);
      { float a[8]; sum8(pb, a); finish(a, 7, bB, hB); }
      { float a[8]; sum8(pc, a); finish(a, 8, bC, hC); }
    }
    TT(3)
    __syncthreads();
    TT(4)
    char* ob = (char*)out + (size_t)(64 * mt + 32 * tp) * (D_ * 2) + wq * 288;
#pragma unroll
    for (int i = 0; i < 3; ++i) {
      const int c = tid + 256 * i;
      if (c < 576) { const int row = c / 18, k = c - 18 * row; const u32x4 ov = *LDSP(const u32x4, ol + row * 304 + k * 16);
        *(u32x4*)(ob + row * (D_ * 2) + k * 16) = ov;
      }
    }
  }
}
extern "C" void kernel_launch(void* const* d_in, const int* in_sizes, int n_in, void* d_out, int out_size, void* d_ws, size_t ws_size,
                              hipStream_t stream) {
  char* ws = (char*)d_ws;
  k1_front<<<K1_N, 256, 0, stream>>>((const u16*)d_in[0], (const u16*)d_in[1], (const u16*)d_in[2], (const u16*)d_in[3],
                                                      (const u16*)d_in[4], (const u16*)d_in[5], (const u16*)d_in[8], (const u16*)d_in[10], ws
  );
  k2_conv<<<K2_N, 256, 0, stream>>>((const u16*)d_in[6], (const u16*)d_in[8], (const u16*)d_in[10], (const u16*)d_in[9], ws);
  k3_mlp<<<256, 256, 0, stream>>>(ws, (const u16*)d_in[10], (const u16*)d_in[9], (const u16*)d_in[11], (u16*)d_out
  );
}
```
